```python
import torch

def _softmax(x, axis=-1):
    return torch.softmax(x, dim=axis)
def _gelu(x):
    return torch.nn.functional.gelu(x, approximate="tanh")
def _silu(x):
    return torch.nn.functional.silu(x)
def _sigmoid(x):
    return torch.sigmoid(x)
def _layernorm(x, axis=-1, eps=1e-5):
    m = x.mean(dim=axis, keepdim=True)
    v = x.var(dim=axis, keepdim=True, unbiased=False)
    return (x - m) / torch.sqrt(v + eps)
def _scatter_add(tgt, idx, src, axis=0):
    return tgt.clone().index_add_(axis, idx, src)
def _segment_sum(src, seg, n):
    out = torch.zeros((n,) + src.shape[1:], dtype=src.dtype)
    return out.index_add_(0, seg, src)
def _pad(x, pw):
    flat = tuple(v for pair in reversed(pw) for v in pair)
    return torch.nn.functional.pad(x, flat)

kernel_name = 'sequential_preLN__sliding_block_cached__glu_gated_swiglu__decode'

def setup_inputs():

    _w = (lambda a: (lambda t: t.to(torch.bfloat16) if t.is_floating_point() else t)(torch.as_tensor(a)))
    def _A_setup():
        B, Sq, D = 2, 1, 576
        Hq, Hkv, Sk, Dh = 32, 16, 32768, 128
        assert Hq % Hkv == 0, (Hq, Hkv)
        return {
            "x": _w((lambda *s: torch.randn(*s, dtype=torch.bfloat16) * 0.05)(B, Sq, D)),
            "k_cache": _w((lambda *s: torch.randn(*s, dtype=torch.bfloat16) * 1.6)(B, Hkv, Sk, Dh)),
            "v_cache": _w((lambda *s: torch.randn(*s, dtype=torch.bfloat16) * 0.8)(B, Hkv, Sk, Dh)),
            "wq": _w((lambda *s: torch.randn(*s, dtype=torch.bfloat16) * 1.6)(D, Hq * Dh) * (D ** -0.5)),
            "wo": _w((lambda *s: torch.randn(*s, dtype=torch.bfloat16) * 0.8)(Hq * Dh, D) * ((Hq * Dh) ** -0.5)),
        }
    _A_in = _A_setup()

    def _B_setup():
        B, S, D, F = 2, 1, 576, 4608
        return {
            "x": _w((lambda *s: torch.randn(*s, dtype=torch.bfloat16) * 0.05)(B, S, D)),
            "w1": _w((lambda *s: torch.randn(*s, dtype=torch.bfloat16) * 0.4)(D, F) * (2.0 / D) ** 0.5),
            "w3": _w((lambda *s: torch.randn(*s, dtype=torch.bfloat16) * 0.4)(D, F) * (2.0 / D) ** 0.5),
            "w2": _w((lambda *s: torch.randn(*s, dtype=torch.bfloat16) * 0.4)(F, D) * (2.0 / F) ** 0.5),
        }
    _B_in = _B_setup()

    return {
        "A_x": _A_in["x"],
        "A_k_cache": _A_in["k_cache"],
        "A_v_cache": _A_in["v_cache"],
        "A_wq": _A_in["wq"],
        "A_wo": _A_in["wo"],
        "B_x": _B_in["x"],
        "B_w1": _B_in["w1"],
        "B_w3": _B_in["w3"],
        "B_w2": _B_in["w2"],
    }

def reference(A_x, A_k_cache, A_v_cache, A_wq, A_wo, B_x, B_w1, B_w3, B_w2):
    def _A_ref(x, k_cache, v_cache, wq, wo):
        B, Sq, D = x.shape
        Hq, Dh = 32, 128
        Hkv, Sk = k_cache.shape[1], k_cache.shape[2]
        W = min(4096, max(256, Sk // 4))
        rep = Hq // Hkv
        q = (lambda x, *d: x.permute(*d))((x @ wq).reshape(B, Sq, Hq, Dh), 0, 2, 1, 3)
        k = (lambda x, n, axis: torch.repeat_interleave(x, n, dim=axis))(k_cache[:, :, Sk - W:, :], rep, axis=1)
        v = (lambda x, n, axis: torch.repeat_interleave(x, n, dim=axis))(v_cache[:, :, Sk - W:, :], rep, axis=1)
        scores = torch.einsum("bhqd,bhkd->bhqk", q, k) * (Dh ** -0.5)
        p = _softmax(scores, axis=-1)
        o = torch.einsum("bhqk,bhkd->bhqd", p, v)
        return (lambda x, *d: x.permute(*d))(o, 0, 2, 1, 3).reshape(B, Sq, Hq * Dh) @ wo

    def _B_ref(x, w1, w3, w2):
        gate = torch.einsum("bsd,df->bsf", x, w1)
        up = torch.einsum("bsd,df->bsf", x, w3)
        h = _silu(gate) * up
        out = torch.einsum("bsf,fd->bsd", h, w2)
        return out

    x = A_x
    h = x + _A_ref(_layernorm(x, axis=-1), A_k_cache, A_v_cache, A_wq, A_wo)
    h2 = h + _B_ref(_layernorm(h, axis=-1), B_w1, B_w3, B_w2)
    return h2
```

```cpp
#include <hip/hip_runtime.h>
#include <hip/hip_bf16.h>
#include <stdint.h>

typedef unsigned short u16;
typedef unsigned v4u __attribute__((ext_vector_type(4)));
constexpr int B_ = 2, D_ = 576, HQ = 32, HKV = 16, SK = 32768, DH = 128, W_ = 4096, F_ = 4608;
constexpr int NSPLIT = 8;

__device__ __forceinline__ float bf2f(unsigned v) { return __builtin_bit_cast(float, v << 16); }
__device__ __forceinline__ float bflo(unsigned v) { return __builtin_bit_cast(float, v << 16); }
__device__ __forceinline__ float bfhi(unsigned v) { return __builtin_bit_cast(float, v & 0xffff0000u); }
__device__ __forceinline__ float rbf(float f) { return __bfloat162float(__float2bfloat16(f)); }

__device__ void ln_to_lds(const float* xin  , float* out, float* red) {}

__device__ __forceinline__ void block_ln(const float* s_in, float* s_x) {
  int t = threadIdx.x; int w = t >> 6, l = t & 63;
  if (w < 2) {
    float v[9]; float s = 0.f;
    for (int i = 0; i < 9; ++i) { v[i] = s_in[w * D_ + l + 64 * i]; s += v[i]; }
    for (int o = 32; o; o >>= 1) s += __shfl_xor(s, o);
    float m = s * (1.f / D_); float q = 0.f;
    for (int i = 0; i < 9; ++i) { float d = v[i] - m; q += d * d; }
    for (int o = 32; o; o >>= 1) q += __shfl_xor(q, o);
    float r = 1.f / sqrtf(q * (1.f / D_) + 1e-5f);
    for (int i = 0; i < 9; ++i) s_x[w * D_ + l + 64 * i] = rbf((v[i] - m) * r);
  }
  __syncthreads();
}

__global__ void __launch_bounds__(256) k_q(const u16* x, const u16* wq, float* q) {
  __shared__ float s_in[2 * D_], s_x[2 * D_], s_r[32][2][64];
  int t = threadIdx.x;
  for (int i = t; i < 2 * D_; i += 256) s_in[i] = bf2f(x[i]);
  __syncthreads();
  block_ln(s_in, s_x);
  int ks = t >> 3, c = t & 7; int n0 = blockIdx.x * 64 + c * 8;
  float a[2][8] = {};
  for (int k = ks; k < D_; k += 32) {
    v4u w = *(const v4u*)(wq + (size_t)k * 4096 + n0);
    float x0 = s_x[k], x1 = s_x[D_ + k];
    for (int j = 0; j < 4; ++j) { float lo = bflo(w[j]), hi = bfhi(w[j]);
      a[0][2*j] += x0 * lo; a[0][2*j+1] += x0 * hi; a[1][2*j] += x1 * lo; a[1][2*j+1] += x1 * hi; }
  }
  for (int j = 0; j < 8; ++j) { s_r[ks][0][c*8+j] = a[0][j]; s_r[ks][1][c*8+j] = a[1][j]; }
  __syncthreads();
  if (t < 128) { int b = t >> 6, n = t & 63; float s = 0.f; for (int i = 0; i < 32; ++i) s += s_r[i][b][n];
    q[b * 4096 + blockIdx.x * 64 + n] = rbf(s); }
}

__global__ void __launch_bounds__(256) k_attn(const float* q, const u16* K, const u16* V, float* po, float* pml) {
  __shared__ float s_s[2][512], s_o[16][2][128], s_red[8];
  int s = blockIdx.x, hk = blockIdx.y, b = blockIdx.z, t = threadIdx.x;
  int c = t & 15, r = t >> 4;
  const u16* kb = K + ((size_t)(b * HKV + hk) * SK + (SK - W_) + s * 512) * DH;
  const u16* vb = V + ((size_t)(b * HKV + hk) * SK + (SK - W_) + s * 512) * DH;
  float q0[8], q1[8];
  for (int j = 0; j < 8; ++j) { q0[j] = q[b * 4096 + (2*hk) * DH + c * 8 + j]; q1[j] = q[b * 4096 + (2*hk+1) * DH + c * 8 + j]; }
  #pragma unroll 8
  for (int i = 0; i < 32; ++i) {
    int key = i * 16 + r;
    v4u w = __builtin_nontemporal_load((const v4u*)(kb + (size_t)key * DH + c * 8));
    float a0 = 0.f, a1 = 0.f;
    for (int j = 0; j < 4; ++j) { float lo = bflo(w[j]), hi = bfhi(w[j]);
      a0 += q0[2*j] * lo + q0[2*j+1] * hi; a1 += q1[2*j] * lo + q1[2*j+1] * hi; }
    for (int o = 8; o; o >>= 1) { a0 += __shfl_xor(a0, o); a1 += __shfl_xor(a1, o); }
    if (c == 0) { s_s[0][key] = a0 * 0.08838834764831845f; s_s[1][key] = a1 * 0.08838834764831845f; }
  }
  __syncthreads();
  float m0 = -INFINITY, m1 = -INFINITY;
  for (int i = t & 63; i < 512; i += 64) { m0 = fmaxf(m0, s_s[0][i]); m1 = fmaxf(m1, s_s[1][i]); }
  for (int o = 32; o; o >>= 1) { m0 = fmaxf(m0, __shfl_xor(m0, o)); m1 = fmaxf(m1, __shfl_xor(m1, o)); }
  __syncthreads();
  for (int i = t; i < 512; i += 256) { s_s[0][i] = __expf(s_s[0][i] - m0); s_s[1][i] = __expf(s_s[1][i] - m1); }
  __syncthreads();
  float l0 = 0.f, l1 = 0.f;
  for (int i = t & 63; i < 512; i += 64) { l0 += s_s[0][i]; l1 += s_s[1][i]; }
  for (int o = 32; o; o >>= 1) { l0 += __shfl_xor(l0, o); l1 += __shfl_xor(l1, o); }
  float o0[8] = {}, o1[8] = {};
  #pragma unroll 8
  for (int i = 0; i < 32; ++i) {
    int key = i * 16 + r;
    v4u w = __builtin_nontemporal_load((const v4u*)(vb + (size_t)key * DH + c * 8));
    float p0 = s_s[0][key], p1 = s_s[1][key];
    for (int j = 0; j < 4; ++j) { float lo = bflo(w[j]), hi = bfhi(w[j]);
      o0[2*j] += p0 * lo; o0[2*j+1] += p0 * hi; o1[2*j] += p1 * lo; o1[2*j+1] += p1 * hi; }
  }
  for (int j = 0; j < 8; ++j) { s_o[r][0][c*8+j] = o0[j]; s_o[r][1][c*8+j] = o1[j]; }
  __syncthreads();
  { int h = t >> 7, d = t & 127; float a = 0.f; for (int i = 0; i < 16; ++i) a += s_o[i][h][d];
    int row = (b * HQ + 2*hk + h);
    po[((size_t)row * NSPLIT + s) * DH + d] = a;
    if (d == 0) { pml[(row * NSPLIT + s) * 2] = h ? m1 : m0; pml[(row * NSPLIT + s) * 2 + 1] = h ? l1 : l0; } }
}

__global__ void __launch_bounds__(128) k_wo(const float* po, const float* pml, const u16* wo, float* ypart) {
  __shared__ float s_o[2][16];
  int j = blockIdx.x, t = threadIdx.x; int h = j >> 3, d0 = (j & 7) * 16;
  if (t < 32) { int b = t >> 4, d = d0 + (t & 15); int row = b * HQ + h;
    float M = -INFINITY; for (int s = 0; s < NSPLIT; ++s) M = fmaxf(M, pml[(row * NSPLIT + s) * 2]);
    float L = 0.f, a = 0.f;
    for (int s = 0; s < NSPLIT; ++s) { float w = __expf(pml[(row * NSPLIT + s) * 2] - M); L += w * pml[(row * NSPLIT + s) * 2 + 1]; a += w * po[((size_t)row * NSPLIT + s) * DH + d]; }
    s_o[b][t & 15] = rbf(a / L); }
  __syncthreads();
  if (t < 72) { float a[2][8] = {};
    for (int r = 0; r < 16; ++r) { v4u w = *(const v4u*)(wo + (size_t)(j * 16 + r) * D_ + t * 8); float x0 = s_o[0][r], x1 = s_o[1][r];
      for (int i = 0; i < 4; ++i) { float lo = bflo(w[i]), hi = bfhi(w[i]);
        a[0][2*i] += x0 * lo; a[0][2*i+1] += x0 * hi; a[1][2*i] += x1 * lo; a[1][2*i+1] += x1 * hi; } }
    for (int i = 0; i < 8; ++i) { ypart[(j * 2 + 0) * D_ + t * 8 + i] = a[0][i]; ypart[(j * 2 + 1) * D_ + t * 8 + i] = a[1][i]; } }
}
__global__ void __launch_bounds__(256) k_red(const float* part, int n, const u16* xb, const float* xf, float* outf, u16* outb) {
  __shared__ float s[8][32];
  int t = threadIdx.x; int col = blockIdx.x * 32 + (t & 31); int g = t >> 5;
  float a = 0.f; for (int i = g; i < n; i += 8) a += part[(size_t)i * 1152 + col];
  s[g][t & 31] = a; __syncthreads();
  if (t < 32) { float r = 0.f; for (int i = 0; i < 8; ++i) r += s[i][t];
    r = rbf(r);
    float base = xb ? bf2f(xb[col]) : xf[col];
    float o = rbf(base + r);
    if (outf) outf[col] = o;
    if (outb) outb[col] = (u16)(__builtin_bit_cast(unsigned, o) >> 16); }
}
__global__ void __launch_bounds__(256) k_ffn(const float* h, const u16* w1, const u16* w3, const u16* w2, float* part) {
  __shared__ float s_in[2 * D_], s_x[2 * D_], s_r[32][2][2][64], s_a[2][64];
  int t = threadIdx.x;
  for (int i = t; i < 2 * D_; i += 256) s_in[i] = h[i];
  __syncthreads();
  block_ln(s_in, s_x);
  int ks = t >> 3, c = t & 7; int n0 = blockIdx.x * 64 + c * 8;
  float a[2][2][8] = {};
  for (int k = ks; k < D_; k += 32) {
    float x0 = s_x[k], x1 = s_x[D_ + k];
    v4u w = *(const v4u*)(w1 + (size_t)k * F_ + n0);
    v4u u = *(const v4u*)(w3 + (size_t)k * F_ + n0);
    for (int j = 0; j < 4; ++j) { float lo = bflo(w[j]), hi = bfhi(w[j]);
      a[0][0][2*j] += x0 * lo; a[0][0][2*j+1] += x0 * hi; a[0][1][2*j] += x1 * lo; a[0][1][2*j+1] += x1 * hi;
      lo = bflo(u[j]); hi = bfhi(u[j]);
      a[1][0][2*j] += x0 * lo; a[1][0][2*j+1] += x0 * hi; a[1][1][2*j] += x1 * lo; a[1][1][2*j+1] += x1 * hi; }
  }
  for (int j = 0; j < 8; ++j) for (int m = 0; m < 2; ++m) for (int b = 0; b < 2; ++b) s_r[ks][m][b][c*8+j] = a[m][b][j];
  __syncthreads();
  if (t < 128) { int b = t >> 6, n = t & 63; float g = 0.f, u = 0.f; for (int i = 0; i < 32; ++i) { g += s_r[i][0][b][n]; u += s_r[i][1][b][n]; }
    g = rbf(g); u = rbf(u); float sl = rbf(g / (1.f + __expf(-g))); s_a[b][n] = rbf(sl * u); }
  __syncthreads();
  if (t < 216) { int rg = t / 72, cc = t % 72; float o[2][8] = {};
    for (int r = rg; r < 64; r += 3) { v4u w = *(const v4u*)(w2 + (size_t)(blockIdx.x * 64 + r) * D_ + cc * 8); float x0 = s_a[0][r], x1 = s_a[1][r];
      for (int i = 0; i < 4; ++i) { float lo = bflo(w[i]), hi = bfhi(w[i]);
        o[0][2*i] += x0 * lo; o[0][2*i+1] += x0 * hi; o[1][2*i] += x1 * lo; o[1][2*i+1] += x1 * hi; } }
    for (int i = 0; i < 8; ++i) { part[((blockIdx.x * 3 + rg) * 2 + 0) * D_ + cc * 8 + i] = o[0][i]; part[((blockIdx.x * 3 + rg) * 2 + 1) * D_ + cc * 8 + i] = o[1][i]; } }
}

extern "C" void kernel_launch(void* const* d_in, const int* in_sizes, int n_in, void* d_out, int out_size, void* d_ws, size_t ws_size, hipStream_t stream) {
  const u16* x = (const u16*)d_in[0]; const u16* K = (const u16*)d_in[1]; const u16* V = (const u16*)d_in[2];
  const u16* wq = (const u16*)d_in[3]; const u16* wo = (const u16*)d_in[4];
  const u16* w1 = (const u16*)d_in[6]; const u16* w3 = (const u16*)d_in[7]; const u16* w2 = (const u16*)d_in[8];
  float* ws = (float*)d_ws;
  float* q = ws; float* po = q + 8192; float* pml = po + 64 * 8 * 128; float* yp = pml + 64 * 8 * 2; float* h = yp + 256 * 1152; float* fp = h + 1152;
  k_q<<<64, 256, 0, stream>>>(x, wq, q);
  k_attn<<<dim3(NSPLIT, HKV, B_), 256, 0, stream>>>(q, K, V, po, pml);
  k_wo<<<256, 128, 0, stream>>>(po, pml, wo, yp);
  k_red<<<36, 256, 0, stream>>>(yp, 256, x, nullptr, h, nullptr);
  k_ffn<<<72, 256, 0, stream>>>(h, w1, w3, w2, fp);
  k_red<<<36, 256, 0, stream>>>(fp, 216, nullptr, h, nullptr, (u16*)d_out);
}
```

```cpp
#include <hip/hip_runtime.h>
#include <stdint.h>

typedef unsigned short u16;
typedef unsigned v4u __attribute__((ext_vector_type(4)));
typedef unsigned v2u __attribute__((ext_vector_type(2)));
typedef float v4f __attribute__((ext_vector_type(4)));
typedef float v2f __attribute__((ext_vector_type(2)));
typedef __bf16 bf8v __attribute__((ext_vector_type(8)));
typedef __bf16 bf2v __attribute__((ext_vector_type(2)));
typedef short s4v __attribute__((ext_vector_type(4)));
typedef __amdgpu_buffer_rsrc_t rsrc_t;
#define GAS __attribute__((address_space(1)))
#define LAS __attribute__((address_space(3)))
#define CAS __attribute__((address_space(4)))
#define DI __device__ __forceinline__

#define NFL 4
#define WOFL 7
#define FFL 6
#define QSTEP 24
#define QSLEEP 12
#define QRETRY 6
#define FDELAY 0
#define W2DELAY 0
#define PSLEEP 30
#define CODE_PIECE 768

constexpr int SK = 32768, WIN = 4096, DM = 576, FF = 4608;
constexpr unsigned TAGM = 0x7ffu;
constexpr int SPIN = 1 << 22;
constexpr int OSTR = 1152, GSTR = 3200;
constexpr int OFF_Q = 4096, OFF_O = OFF_Q + 256 * 512, OFF_Y = OFF_O + 256 * OSTR, OFF_G = OFF_Y + 8 * 4608, OFF_P = OFF_G + 144 * GSTR, WS_END = OFF_P + 36 * 4608;
constexpr int L_OB = 147456, L_ML = L_OB + 8192, L_QB = L_ML + 128, L_ACT = L_QB + 512, L_FLG = L_ACT + 2048, L_END = L_FLG + 64;
enum { F_ISS = 0, F_DONE = 1, F_QCNT = 2, F_QRDY = 3, F_WL = 4 };

DI float asf(unsigned v) { return __builtin_bit_cast(float, v); }
DI unsigned asu(float v) { return __builtin_bit_cast(unsigned, v); }
DI unsigned pkbf(float a, float b) { v2f f = {a, b}; return __builtin_bit_cast(unsigned, __builtin_convertvector(f, bf2v)); }
DI float lo16(unsigned v) { return asf(v << 16); }
DI float hi16(unsigned v) { return asf(v & 0xffff0000u); }
DI float rbf(float a) { return lo16(pkbf(a, a)); }
template <int C> DI unsigned dppu(unsigned v) { return (unsigned)__builtin_amdgcn_update_dpp(0, (int)v, C, 0xf, 0xf, true); }
template <int C> DI float dppf(float v) { return asf(dppu<C>(asu(v))); }
constexpr int QX1 = 0xB1, QX2 = 0x4E, HMIR = 0x141, RMIR = 0x140, ROR8 = 0x128, NBC = 0x150;
DI float sw32add(float a, float b) { v2u r = __builtin_amdgcn_permlane32_swap(asu(a), asu(b), false, false); return asf(r[0]) + asf(r[1]); }
DI float sw16add(float a, float b) { v2u r = __builtin_amdgcn_permlane16_swap(asu(a), asu(b), false, false); return asf(r[0]) + asf(r[1]); }
DI float sw32max(float a) { v2u r = __builtin_amdgcn_permlane32_swap(asu(a), asu(a), false, false); return fmaxf(asf(r[0]), asf(r[1])); }
DI float sw16max(float a) { v2u r = __builtin_amdgcn_permlane16_swap(asu(a), asu(a), false, false); return fmaxf(asf(r[0]), asf(r[1])); }
DI float rowscatter(float v0, float v1, float v2, float v3) { return sw16add(sw32add(v0, v1), sw32add(v2, v3)); }
DI float rowsum16(float s) { s += dppf<QX1>(s); s += dppf<QX2>(s); s += dppf<HMIR>(s); s += dppf<RMIR>(s); return s; }
DI v4f mfma(v4u a, v4u b, v4f c) { return __builtin_amdgcn_mfma_f32_16x16x32_bf16(__builtin_bit_cast(bf8v, a), __builtin_bit_cast(bf8v, b), c, 0, 0, 0); }
DI unsigned tg(float v, unsigned tag) { return (asu(v) & ~TAGM) | tag; }
DI float untag(unsigned v) { return asf(v & ~TAGM); }
DI bool bad4(v4u v, unsigned tag) { return (((v.x ^ tag) | (v.y ^ tag) | (v.z ^ tag) | (v.w ^ tag)) & TAGM) != 0; }
DI bool anyl(bool b) { return __builtin_amdgcn_ballot_w64(b) != 0; }
DI v4u ldc(rsrc_t rs, int voff, int soff) { return __builtin_amdgcn_raw_buffer_load_b128(rs, voff, soff, 16); }
DI void stc(v4u v, rsrc_t rs, int voff, int soff) { __builtin_amdgcn_raw_buffer_store_b128(v, rs, voff, soff, 16); }
DI v4u ldnt(const GAS char* base, unsigned off) { return __builtin_nontemporal_load((const GAS v4u*)(base + off)); }
#define WAITV(n) asm volatile("s_waitcnt vmcnt(%0)" ::"n"(n) : "memory")
#define WAITL() asm volatile("s_waitcnt lgkmcnt(0)" ::: "memory")
#define SB() __builtin_amdgcn_sched_barrier(0)
#define MEMB() asm volatile("" ::: "memory")
#define L4(o) (*(LAS v4u*)(lds + (o)))
#define L2_(o) (*(LAS v2u*)(lds + (o)))
DI void glds(const GAS char* sbase, unsigned voff, unsigned lds_dst) {
  unsigned keep;
  asm volatile("s_mov_b32 %0, m0\n\ts_mov_b32 m0, %3\n\ts_nop 0\n\tglobal_load_lds_dwordx4 %1, %2 nt\n\ts_mov_b32 m0, %0" : "=&s"(keep) : "v"(voff), "s"(sbase), "s"(lds_dst) : "memory");
}
DI v4u trfrag(LAS char* lds, unsigned a) {
  s4v r0 = __builtin_amdgcn_ds_read_tr16_b64_v4i16((LAS s4v*)(lds + a));
  s4v r1 = __builtin_amdgcn_ds_read_tr16_b64_v4i16((LAS s4v*)(lds + a + 512));
  v2u x = __builtin_bit_cast(v2u, r0), y = __builtin_bit_cast(v2u, r1);
  return v4u{x.x, x.y, y.x, y.y};
}
DI unsigned lflag(LAS char* lds, int f) { return *(volatile LAS unsigned*)(lds + L_FLG + 4 * f); }
DI unsigned larrive(LAS char* lds, int f, int l) {
  unsigned old = 0;
  WAITL();
  if (l == 0) old = __hip_atomic_fetch_add((LAS unsigned*)(lds + L_FLG + 4 * f), 1u, __ATOMIC_RELAXED, __HIP_MEMORY_SCOPE_WORKGROUP);
  old = __builtin_amdgcn_readfirstlane(old);
  MEMB();
  return old;
}
template <int SL> DI void lwait(LAS char* lds, int f, unsigned n) { while (lflag(lds, f) < n) { if (SL) __builtin_amdgcn_s_sleep(SL); } MEMB(); }
DI void barrier() { WAITL(); __builtin_amdgcn_s_barrier(); MEMB(); }

#define STAMP(i) do {} while (0)

DI float gather_h(rsrc_t rs, const u16* x, int cg, int l, unsigned tag) {
  const int rho = l >> 4, e = ((rho & 1) << 1) | (rho >> 1);
  const float xv = lo16(x[((l >> 2) & 1) * DM + cg * 16 + (l & 3) * 4 + e]);
  const int off = OFF_Y + (l >> 3) * 4608 + cg * 128 + (l & 7) * 16;
  v4u g = {0, 0, 0, 0};
  bool bad = true;
  for (int s = 0; s < SPIN; ++s) {
    MEMB();
    if (bad) { g = ldc(rs, off, 0); bad = bad4(g, tag); }
    if (!anyl(bad)) break;
  }
  float t = rowscatter(untag(g.x), untag(g.y), untag(g.z), untag(g.w));
  t += dppf<ROR8>(t);
  return rbf(xv + rbf(t));
}

__global__ void __launch_bounds__(768, 3) k_fused(const u16* x, const u16* K, const u16* wq, unsigned* ws, const u16* V, const u16* wo, const u16* w1, const u16* w3,
                                                  const u16* w2, u16* out
) {
  extern __shared__ __attribute__((aligned(16))) char smem[];
  LAS char* lds = (LAS char*)smem;
  const int blk = blockIdx.x, t = threadIdx.x, w = __builtin_amdgcn_readfirstlane(t >> 6), l = t & 63;
  const int xcd = blk & 7, idx = blk >> 3;
  v4u pf = {0, 0, 0, 0};
  {
    unsigned long long pc; unsigned len;
    asm volatile("s_getpc_b64 %0\n1:\n\ts_mov_b32 %1, .Lkf_last-1b" : "=s"(pc), "=s"(len));
    const unsigned po = idx * CODE_PIECE + l * 16;
    if (w == 3 && l < CODE_PIECE / 16 && po + 16 <= len) asm volatile("global_load_dwordx4 %0, %1, %2" : "=v"(pf) : "v"(po), "s"(pc) : "memory");
  }
  const int hk = idx & 15, bb = idx >> 4, qi = xcd + 8 * bb;
  const int kw = w - 4, rot = (blk * 5 + idx * 3) & 15;
  const size_t kvb = (((size_t)idx * SK + (SK - WIN) + xcd * 512) * 128 + kw * 512) * 2;
  const GAS char* kp = (const GAS char*)K + kvb;
  const unsigned koff = ((l >> 2) & 3) * 256 + ((l >> 4) * 4 + (l & 3)) * 16;
  v4u a[32];
  v4u xa, xb; unsigned xc, xp[5];
  const int qrow = 144 * (qi >> 2) + 8 * w;
  if (w < 4) {
    const GAS char* xs = (const GAS char*)x;
    xa = *(const GAS v4u*)(xs + l * 36); xb = *(const GAS v4u*)(xs + l * 36 + 16); xc = *(const GAS unsigned*)(xs + l * 36 + 32);
    const GAS char* qp = (const GAS char*)wq + ((size_t)qrow * 4096 + hk * 256 + (qi & 3) * 64) * 2;
    const unsigned qoff = (l >> 3) * 8192 + (l & 7) * 16;
#pragma unroll
    for (int i = 0; i < 4; ++i) a[i] = ldnt(qp + i * 32 * 8192, qoff);
    a[4] = ldnt(qp + (w < 2 ? 4 * 32 * 8192 : 0), qoff);
    const unsigned xo = (((l >> 3) & 1) * DM + qrow + 2 * (l >> 4)) * 2;
#pragma unroll
    for (int i = 0; i < 5; ++i) xp[i] = *(const GAS unsigned*)(xs + xo + ((i < 4 || w < 2) ? i * 64 : 0));
  } else {
#pragma unroll
    for (int i = 0; i < NFL; ++i) a[i] = ldnt(kp + ((i + rot) & 15) * 8192, koff);
  }
  if (t < 16) *(LAS unsigned*)(lds + L_FLG + 4 * t) = 0;
  const unsigned ep = *(const CAS unsigned*)ws;
  barrier();
  const unsigned tag = (ep + 1) & TAGM;
  const rsrc_t rs = __builtin_amdgcn_make_buffer_rsrc((void*)ws, 0, WS_END, 0x00020000);
  const int rho = (idx + 4 * xcd) & 31;
  const bool isWo = rho < 9, isG = rho >= 9 && rho < 27;
  const int gi = (rho - 9) * 8 + xcd, kq = gi & 3, cb = gi >> 2;
  const int w2i = (rho - 27) * 8 + xcd;
  const bool isW2 = rho >= 27 && w2i < 36;
  const int g4 = l >> 4, n16 = l & 15;
  unsigned mk[4];
#pragma unroll
  for (int r = 0; r < 4; ++r) mk[r] = ((n16 & 7) >> 1) == r ? ((n16 & 1) ? 0xffff0000u : 0x0000ffffu) : 0u;
  constexpr float LOG2E = 1.4426950408889634f;

  if (w >= 4) {
    __builtin_amdgcn_s_setprio(1);
    const GAS char* vp = (const GAS char*)V + kvb;
    const unsigned voff = l * 16;
    v4u q0, q1; float sc[8]; unsigned pp[8]; float mx = 0.f, ls = 0.f; v4f oa = {0.f, 0.f, 0.f, 0.f};
    auto qk = [&](int i) {
      v4f d = mfma(a[2 * i], q0, v4f{0.f, 0.f, 0.f, 0.f});
      d = mfma(a[2 * i + 1], q1, d);
      sc[i] = ((dppf<0x00>(d[0]) + dppf<0x55>(d[1])) + dppf<0xAA>(d[2])) + dppf<0xFF>(d[3]);
    };
    auto pv = [&](int j) {
      const unsigned own = pp[j >> 1], oth = dppu<HMIR>(own);
      const unsigned x0 = (j & 1) ? oth : own, x1 = (j & 1) ? own : oth;
      oa = mfma(v4u{x0 & mk[0], x0 & mk[1], x1 & mk[2], x1 & mk[3]}, a[16 + j], oa);
    };
#pragma unroll
    for (int i = NFL; i < 32; ++i) {
      WAITV(NFL - 1);
      a[i] = ldnt((i < 16 ? kp : vp) + ((i + rot) & 15) * 8192, i < 16 ? koff : voff);
      SB();
      if (i == 31) { if (l == 0) __hip_atomic_fetch_add((LAS unsigned*)(lds + L_FLG + 4 * F_ISS), 1u, __ATOMIC_RELAXED, __HIP_MEMORY_SCOPE_WORKGROUP); }
      if (i == QSTEP) {
        lwait<1>(lds, F_QRDY, 1);
        const v4u qf = L4(L_QB + ((n16 >> 3) * 128 + 32 * g4 + 8 * (n16 & 3)) * 2);
        const bool x1 = (n16 >> 2) & 1;
        const v4u z = {0, 0, 0, 0};
        q0 = x1 ? z : qf; q1 = x1 ? qf : z;
#pragma unroll
        for (int p = 0; p < 4; ++p) qk(p);
      }
      if (i == QSTEP + 1) {
#pragma unroll
        for (int p = 4; p < 8; ++p) qk(p);
      }
      if (i == QSTEP + 2) {
#pragma unroll
        for (int p = 0; p < 8; p += 2) {
          const unsigned u = pkbf(sc[p], sc[p + 1]);
          const unsigned v = pkbf(lo16(u) * 0.08838834764831845f, hi16(u) * 0.08838834764831845f);
          sc[p] = lo16(v); sc[p + 1] = hi16(v);
        }
        mx = fmaxf(fmaxf(fmaxf(sc[0], sc[1]), fmaxf(sc[2], sc[3])), fmaxf(fmaxf(sc[4], sc[5]), fmaxf(sc[6], sc[7])));
        mx = fmaxf(mx, dppf<HMIR>(mx)); mx = sw16max(mx); mx = sw32max(mx);
      }
      if (i == QSTEP + 3) {
#pragma unroll
        for (int p = 0; p < 8; ++p) { const float e = __builtin_amdgcn_exp2f((sc[p] - mx) * LOG2E); ls += e; pp[p] = pkbf(e, e); }
        ls += dppf<HMIR>(ls); ls = sw16add(ls, ls); ls = sw32add(ls, ls);
        if ((l & 55) == 0) L2_(L_ML + kw * 16 + (l >> 3) * 8) = v2u{asu(mx), asu(ls)};
      }
      if (i >= QSTEP + 4) {
        const int s = i - (QSTEP + 4), ns = 32 - (QSTEP + 4), tot = 32 - 16 - NFL;
#pragma unroll
        for (int j = tot * s / ns; j < tot * (s + 1) / ns; ++j) pv(j);
      }
      SB();
    }
#pragma unroll
    for (int j = 16 - NFL; j < 16; ++j) pv(j);
    L4(L_OB + kw * 1024 + l * 16) = __builtin_bit_cast(v4u, oa);
    const unsigned old = larrive(lds, F_DONE, l);
    if (old == 7) {
      STAMP(13);
      v4u o8[8]; v2u ml[8];
#pragma unroll
      for (int i = 0; i < 8; ++i) { ml[i] = L2_(L_ML + i * 16 + (l >> 5) * 8); o8[i] = L4(L_OB + i * 1024 + l * 16); }
      float M = asf(ml[0].x);
#pragma unroll
      for (int i = 1; i < 8; ++i) M = fmaxf(M, asf(ml[i].x));
      float L = 0.f; v4f o = {0.f, 0.f, 0.f, 0.f};
#pragma unroll
      for (int i = 0; i < 8; ++i) { const float e = __builtin_amdgcn_exp2f((asf(ml[i].x) - M) * LOG2E); L = fmaf(e, asf(ml[i].y), L);
#pragma unroll
        for (int c = 0; c < 4; ++c) o[c] = fmaf(e, asf(o8[i][c]), o[c]); }
      stc(v4u{tg(o[0], tag), tg(o[1], tag), tg(o[2], tag), tg(o[3], tag)}, rs, OFF_O + blk * OSTR + l * 16, 0);
      if ((l & 31) == 0) __builtin_amdgcn_raw_buffer_store_b64(v2u{tg(M, tag), tg(L, tag)}, rs, OFF_O + blk * OSTR + 1024 + (l >> 5) * 8, 0, 16);
      STAMP(14);
    }
    __builtin_amdgcn_s_setprio(0);
  } else {
    __builtin_amdgcn_s_setprio(3);
    float xv[18];
#pragma unroll
    for (int i = 0; i < 4; ++i) { xv[2 * i] = lo16(xa[i]); xv[2 * i + 1] = hi16(xa[i]); xv[8 + 2 * i] = lo16(xb[i]); xv[9 + 2 * i] = hi16(xb[i]); }
    xv[16] = lo16(xc); xv[17] = hi16(xc);
    float s = 0.f;
#pragma unroll
    for (int i = 0; i < 18; ++i) s += xv[i];
    s = rowsum16(s); s = sw16add(s, s);
    const float mean = s * (1.f / DM);
    float vs = 0.f;
#pragma unroll
    for (int i = 0; i < 18; ++i) { const float d = xv[i] - mean; vs = fmaf(d, d, vs); }
    vs = rowsum16(vs); vs = sw16add(vs, vs);
    const float mr = rbf(mean), dn = rbf(__builtin_amdgcn_sqrtf(rbf(rbf(vs * (1.f / DM)) + 1e-5f)));
    const bool b1 = (l >> 3) & 1;
    const unsigned m0_ = __builtin_amdgcn_readlane(asu(mr), 0), m1_ = __builtin_amdgcn_readlane(asu(mr), 32);
    const unsigned d0_ = __builtin_amdgcn_readlane(asu(dn), 0), d1_ = __builtin_amdgcn_readlane(asu(dn), 32);
    const float mS = asf(b1 ? m1_ : m0_), dS = asf(b1 ? d1_ : d0_);
    const float rS = __builtin_amdgcn_rcpf(dS);
    v4f de = {0.f, 0.f, 0.f, 0.f}, dd = {0.f, 0.f, 0.f, 0.f};
#pragma unroll
    for (int i = 0; i < 5; ++i) {
      const unsigned u = pkbf(lo16(xp[i]) - mS, hi16(xp[i]) - mS);
      const float n0 = lo16(u), n1 = hi16(u);
      float q0 = n0 * rS, q1 = n1 * rS;
      q0 = fmaf(fmaf(-q0, dS, n0), rS, q0); q1 = fmaf(fmaf(-q1, dS, n1), rS, q1);
      unsigned pe = pkbf(q0, q0), po = pkbf(q1, q1);
      if (i == 4 && w >= 2) { pe = 0; po = 0; }
      de = mfma(v4u{pe & mk[0], pe & mk[1], pe & mk[2], pe & mk[3]}, a[i], de);
      dd = mfma(v4u{po & mk[0], po & mk[1], po & mk[2], po & mk[3]}, a[i], dd);
    }
    if (w == 0) STAMP(15);
    v4f r;
#pragma unroll
    for (int c = 0; c < 4; ++c) r[c] = de[c] + dppf<ROR8>(dd[c]);
    if (n16 < 8) L4(L_OB + ((w * 2 + (g4 >> 1)) * 64 + 8 * n16 + 4 * (g4 & 1)) * 4) = __builtin_bit_cast(v4u, r);
    asm volatile("" ::"v"(pf));
    const unsigned old = larrive(lds, F_QCNT, l);
    if (old == 3) {
      v4f p[4];
#pragma unroll
      for (int i = 0; i < 4; ++i) p[i] = __builtin_bit_cast(v4f, L4(L_OB + i * 512 + (l & 31) * 16));
      const v4f q = (p[0] + p[1]) + (p[2] + p[3]);
      if (l < 32) stc(v4u{tg(q[0], tag), tg(q[1], tag), tg(q[2], tag), tg(q[3], tag)}, rs, OFF_Q + (hk * 16 + qi) * 512 + l * 16, 0);
      STAMP(5);
      if (QSLEEP) __builtin_amdgcn_s_sleep(QSLEEP);
      const int off = OFF_Q + (hk * 16 + g4) * 512 + bb * 256 + n16 * 16;
      v4u g[4]; bool bad[4] = {true, true, true, true};
      for (int sp_ = 0; sp_ < SPIN; ++sp_) {
        MEMB();
#pragma unroll
        for (int j = 0; j < 4; ++j) g[j] = ldc(rs, bad[j] ? off : WS_END, j * 2048) | (bad[j] ? v4u{0, 0, 0, 0} : g[j]);
#pragma unroll
        for (int j = 0; j < 4; ++j) bad[j] = bad4(g[j], tag);
        if (!anyl(bad[0] | bad[1] | bad[2] | bad[3])) break;
        if (QRETRY) __builtin_amdgcn_s_sleep(QRETRY);
      }
      float qv[4];
#pragma unroll
      for (int c = 0; c < 4; ++c) qv[c] = (untag(g[0][c]) + untag(g[1][c])) + (untag(g[2][c]) + untag(g[3][c]));
      L2_(L_QB + l * 8) = v2u{pkbf(qv[0], qv[1]), pkbf(qv[2], qv[3])};
      WAITL();
      if (l == 0) *(volatile LAS unsigned*)(lds + L_FLG + 4 * F_QRDY) = 1;
      STAMP(1);
    }
    __builtin_amdgcn_s_setprio(0);
    if (isWo || isG || isW2) {
      const unsigned fsw = (((l >> 4) & 1) | ((w & 1) << 1)) << 1;
      const unsigned ld = isG ? FF * 2 : DM * 2;
      const unsigned vo = (l >> 3) * ld + (((l & 7) ^ fsw) * 16);
      if (isWo) {
        lwait<2>(lds, F_ISS, 8);
        const GAS char* src = (const GAS char*)wo + ((size_t)(xcd * 512 + w * 8) * DM + rho * 64) * 2;
        for (int j = 0; j < 16; ++j) { glds(src + j * (32 * DM * 2), vo, (w + 4 * j) * 1024); WAITV(WOFL - 1); }
      } else if (isG) {
        lwait<2>(lds, F_DONE, 8);
        if (FDELAY) __builtin_amdgcn_s_sleep(FDELAY);
        for (int j = 0; j < 18; ++j) {
          const int e = (w >> 1) + 2 * j, rg = (w & 1) + 2 * (e >> 2), im = e & 3;
          const GAS char* src = (const GAS char*)((im & 2) ? w3 : w1) + ((size_t)(kq * 144 + rg * 8) * FF + cb * 128 + (im & 1) * 64) * 2;
          glds(src, vo, im * 18432 + rg * 1024); WAITV(FFL - 1);
        }
      } else {
        lwait<2>(lds, F_DONE, 8);
        if (W2DELAY) __builtin_amdgcn_s_sleep(W2DELAY);
        for (int j = 0; j < 36; ++j) {
          const int e = (w >> 1) + 2 * j, aa = e / 9, cg = e - 9 * aa, rg = (w & 1) + 2 * aa;
          const GAS char* src = (const GAS char*)w2 + ((size_t)(w2i * 128 + rg * 8) * DM + cg * 64) * 2;
          glds(src, vo, cg * 16384 + rg * 1024); WAITV(FFL - 1);
        }
      }
      WAITV(0);
      larrive(lds, F_WL, l);
      if (w == 0) STAMP(isWo ? 3 : isG ? 4 : 2);
    }
  }

  const int tq = (l >> 2) & 3, tp = l & 3;
  const unsigned trf = ((tq >> 1) | ((g4 & 1) << 1)) << 1;
  const unsigned trow = (8 * g4 + tq) * 128 + (tp & 1) * 8;
  auto traddr = [&](int tt) { return trow + (((2 * tt + (tp >> 1)) ^ trf) * 16); };
  if (isWo) {
    v4u wf[16];
    if (w < 4) {
      lwait<1>(lds, F_WL, 4);
      const unsigned ad = traddr(w);
#pragma unroll
      for (int ks = 0; ks < 16; ++ks) wf[ks] = trfrag(lds, ad + ks * 4096);
    } else if (w < 8) {
      const int w4 = w - 4, pb = ((w4 >> 1) * 16 + 2 * xcd + (w4 & 1)) * 8;
      const int so = OFF_O + pb * OSTR;
      const int mlo = so + (l & 7) * OSTR + 1024 + (l >> 5) * 8;
      v4u g[8]; v2u ml;
#pragma unroll
      for (int j = 0; j < 8; ++j) g[j] = v4u{0, 0, 0, 0};
      unsigned pend = 0xff, rdy = 0; bool mlbad = true;
      for (int s = 0; s < SPIN; ++s) {
        const unsigned go = __builtin_popcount(pend) <= 2 ? pend : (pend & rdy);
        asm volatile("buffer_load_dwordx2 %0, %1, %2, 0 offen sc1" : "=v"(ml) : "v"(mlo), "s"(rs) : "memory");
#pragma unroll
        for (int j = 0; j < 8; ++j) if (go >> j & 1) asm volatile("buffer_load_dwordx4 %0, %1, %2, %3 offen sc1" : "+v"(g[j]) : "v"(l * 16), "s"(rs), "s"(so + j * OSTR) : "memory");
        asm volatile("s_waitcnt vmcnt(0)" : "+v"(ml), "+v"(g[0]), "+v"(g[1]), "+v"(g[2]), "+v"(g[3]), "+v"(g[4]), "+v"(g[5]), "+v"(g[6]), "+v"(g[7])::"memory");
        mlbad = (((ml.x ^ tag) | (ml.y ^ tag)) & TAGM) != 0;
        const unsigned long long bm = __builtin_amdgcn_ballot_w64(mlbad);
        rdy = ~((unsigned)bm | (unsigned)(bm >> 32)) & 0xff;
#pragma unroll
        for (int j = 0; j < 8; ++j) if (go >> j & 1) { if (!anyl(bad4(g[j], tag))) pend &= ~(1u << j); }
        if (pend == 0 && rdy == 0xff) break;
      }
      if (w == 4) STAMP(6);
      const float Ms = untag(ml.x);
      float M = fmaxf(Ms, dppf<QX1>(Ms)); M = fmaxf(M, dppf<QX2>(M)); M = fmaxf(M, dppf<HMIR>(M));
      const float e = __builtin_amdgcn_exp2f((Ms - M) * LOG2E);
      float L = e * untag(ml.y); L += dppf<QX1>(L); L += dppf<QX2>(L); L += dppf<HMIR>(L);
      const float wt = e * __builtin_amdgcn_rcpf(L);
      float o[4] = {0.f, 0.f, 0.f, 0.f};
#define OACC(j) { const float ww = dppf<NBC + j>(wt); for (int c = 0; c < 4; ++c) o[c] = fmaf(ww, untag(g[j][c]), o[c]); }
      OACC(0) OACC(1) OACC(2) OACC(3) OACC(4) OACC(5) OACC(6) OACC(7)
#undef OACC
      L2_(L_ACT + ((w4 >> 1) * 512 + (w4 & 1) * 256 + (l >> 5) * 128 + 8 * n16 + 4 * (g4 & 1)) * 2) = v2u{pkbf(o[0], o[1]), pkbf(o[2], o[3])};
    }
    barrier();
    if (w < 4) {
      v4f d0 = {0.f, 0.f, 0.f, 0.f}, d1 = {0.f, 0.f, 0.f, 0.f};
      const unsigned ba = L_ACT + (n16 & 1) * 1024 + g4 * 16;
#pragma unroll
      for (int ks = 0; ks < 16; ks += 2) { d0 = mfma(wf[ks], L4(ba + ks * 64), d0); d1 = mfma(wf[ks + 1], L4(ba + ks * 64 + 64), d1); }
      const v4f d = d0 + d1;
      if (n16 < 2) stc(v4u{tg(d[0], tag), tg(d[1], tag), tg(d[2], tag), tg(d[3], tag)}, rs, OFF_Y + xcd * 4608 + (rho * 4 + w) * 128 + n16 * 64 + g4 * 16, 0);
      if (w == 0) STAMP(7);
    } else if (w == 8 && rho * 8 + xcd < 36) {
      const int r = rho * 8 + xcd;
      const float h = gather_h(rs, x, r, l, tag);
      if (PSLEEP) __builtin_amdgcn_s_sleep(PSLEEP);
      const int off = OFF_P + (l >> 3) * 4608 + r * 128 + (l & 7) * 16;
      v4u g[5];
      for (int s = 0; s < SPIN; ++s) {
        MEMB();
#pragma unroll
        for (int j = 0; j < 4; ++j) g[j] = ldc(rs, off, j * 8 * 4608);
        g[4] = ldc(rs, l < 32 ? off : WS_END, 32 * 4608);
        const bool bad = bad4(g[0], tag) | bad4(g[1], tag) | bad4(g[2], tag) | bad4(g[3], tag) | (l < 32 && bad4(g[4], tag));
        if (!anyl(bad)) break;
      }
      float v[4];
#pragma unroll
      for (int c = 0; c < 4; ++c) v[c] = ((untag(g[0][c]) + untag(g[1][c])) + (untag(g[2][c]) + untag(g[3][c]))) + untag(g[4][c]);
      float o = rowscatter(v[0], v[1], v[2], v[3]);
      o += dppf<ROR8>(o);
      const unsigned res = pkbf(h + rbf(o), 0.f);
      const int e = ((g4 & 1) << 1) | (g4 >> 1);
      if (!(l & 8)) out[((l >> 2) & 1) * DM + r * 16 + (l & 3) * 4 + e] = (u16)res;
      if (r == 0 && l == 0) __builtin_amdgcn_raw_buffer_store_b32(ep + 1, rs, 0, 0, 16);
      STAMP(12);
    }
  } else if (isG) {
    v4u wf[4][5]; v4f cs[4];
    if (w == 4) { if (l < 32) *(LAS u16*)(lds + L_ACT + ((l >> 4) * 160 + 144 + (l & 15)) * 2) = 0; }
    lwait<2>(lds, F_WL, 4);
    if (w < 4) {
      const v4u ones = {0x3f803f80u, 0x3f803f80u, 0x3f803f80u, 0x3f803f80u};
      const v4u onest = g4 < 2 ? ones : v4u{0, 0, 0, 0};
#pragma unroll
      for (int u = 0; u < 4; ++u) {
        const int T = 2 * w + (u >> 1);
        const unsigned ad = ((u & 1) * 2 + (T >> 2)) * 18432 + traddr(T & 3);
#pragma unroll
        for (int ks = 0; ks < 4; ++ks) wf[u][ks] = trfrag(lds, ad + ks * 4096);
        wf[u][4] = trfrag(lds, ad + 4 * 4096 - (g4 >= 2 ? 2048 : 0));
        v4f c = {0.f, 0.f, 0.f, 0.f};
#pragma unroll
        for (int ks = 0; ks < 5; ++ks) c = mfma(wf[u][ks], ks < 4 ? ones : onest, c);
        cs[u] = c;
        if (n16 == 0) stc(v4u{tg(c[0], tag), tg(c[1], tag), tg(c[2], tag), tg(c[3], tag)}, rs, OFF_G + gi * GSTR + 2176 + ((T * 2 + (u & 1)) * 4 + g4) * 16, 0);
      }
    }
    if (w >= 3) {
      const float h = gather_h(rs, x, kq * 9 + (w - 3), l, tag);
      const int e = ((g4 & 1) << 1) | (g4 >> 1);
      if (!(l & 8)) *(LAS u16*)(lds + L_ACT + (((l >> 2) & 1) * 160 + (w - 3) * 16 + (l & 3) * 4 + e) * 2) = (u16)(asu(h) >> 16);
      if (w == 4) STAMP(8);
    }
    barrier();
    if (w < 4) {
      const unsigned ba = L_ACT + (n16 & 1) * 320;
      v4u hb[5];
#pragma unroll
      for (int ks = 0; ks < 5; ++ks) hb[ks] = L4(ba + g4 * 16 + ks * 64);
      const float c0 = lo16(*(LAS u16*)(lds + ba));
      v4f d[4];
#pragma unroll
      for (int u = 0; u < 4; ++u) d[u] = v4f{0.f, 0.f, 0.f, 0.f};
#pragma unroll
      for (int ks = 0; ks < 5; ++ks)
#pragma unroll
        for (int u = 0; u < 4; ++u) d[u] = mfma(wf[u][ks], hb[ks], d[u]);
#pragma unroll
      for (int u = 0; u < 4; ++u) {
        v4u o;
#pragma unroll
        for (int c = 0; c < 4; ++c) o[c] = tg(fmaf(-c0, cs[u][c], d[u][c]), tag);
        if (n16 < 2) stc(o, rs, OFF_G + gi * GSTR + (((2 * w + (u >> 1)) * 2 + (u & 1)) * 8 + n16 * 4 + g4) * 16, 0);
      }
      if (w == 0) STAMP(9);
    } else if (w == 4) {
      const unsigned ba = L_ACT + (l >> 5) * 320;
      const bool on = (l & 31) < 18;
      const v4u hv = L4(ba + (on ? (l & 31) : 0) * 16);
      const float c0 = lo16(*(LAS u16*)(lds + ba));
      float s = 0.f, q = 0.f;
#pragma unroll
      for (int i = 0; i < 4; ++i) { const float d0 = lo16(hv[i]) - c0, d1 = hi16(hv[i]) - c0; s += d0 + d1; q = fmaf(d0, d0, fmaf(d1, d1, q)); }
      if (!on) { s = 0.f; q = 0.f; }
      s = rowsum16(s); q = rowsum16(q); s = sw16add(s, s); q = sw16add(q, q);
      const float dl = s * (1.f / 144.f), m2 = fmaxf(fmaf(-s, dl, q), 0.f);
      if ((l & 31) == 0) stc(v4u{tg(c0, tag), tg(dl, tag), tg(m2, tag), tag}, rs, OFF_G + gi * GSTR + 2048 + (l >> 5) * 16, 0);
    }
  } else if (isW2) {
    v4u wf[3][4];
    lwait<2>(lds, F_WL, 4);
#pragma unroll
    for (int u = 0; u < 3; ++u) {
      const int T = 3 * w + u;
      const unsigned ad = (T >> 2) * 16384 + traddr(T & 3);
#pragma unroll
      for (int ks = 0; ks < 4; ++ks) wf[u][ks] = trfrag(lds, ad + ks * 4096);
    }
    if (w >= 4) {
      const int T = w - 4, pbase = OFF_G + (4 * w2i + g4) * GSTR;
      v4u cv;
      for (int s = 0; s < SPIN; ++s) { MEMB(); cv = ldc(rs, pbase + 2176 + ((T * 2 + ((l >> 3) & 1)) * 4 + (l & 3)) * 16, 0); if (!anyl(bad4(cv, tag))) break; }
      const float CS = rowscatter(untag(cv.x), untag(cv.y), untag(cv.z), untag(cv.w));
      v4u g, st;
      for (int s = 0; s < SPIN; ++s) {
        MEMB();
        g = ldc(rs, pbase + T * 256 + n16 * 16, 0); st = ldc(rs, pbase + 2048 + ((l >> 2) & 1) * 16, 0);
        if (!anyl(bad4(g, tag) | bad4(st, tag))) break;
      }
      if (w == 4) STAMP(10);
      const float c0 = untag(st.x), mq = c0 + untag(st.y);
      float m = sw16add(mq, mq); m = sw32add(m, m); m *= 0.25f;
      const float dm = mq - m;
      float m2 = fmaf(144.f * dm, dm, untag(st.z)); m2 = sw16add(m2, m2); m2 = sw32add(m2, m2);
      const float mr = rbf(m), dn = rbf(__builtin_amdgcn_sqrtf(rbf(rbf(m2 * (1.f / DM)) + 1e-5f)));
      float v[4];
#pragma unroll
      for (int c = 0; c < 4; ++c) v[c] = fmaf(c0, untag(cv[c]), untag(g[c]));
      float val = rowscatter(v[0], v[1], v[2], v[3]);
      val = rbf(fmaf(-mr, CS, val) * __builtin_amdgcn_rcpf(dn));
      const float up = dppf<ROR8>(val);
      const float sl = rbf(val * __builtin_amdgcn_rcpf(1.f + __builtin_amdgcn_exp2f(-val * LOG2E)));
      const int e = ((g4 & 1) << 1) | (g4 >> 1);
      if (!(l & 8)) *(LAS u16*)(lds + L_ACT + (((l >> 2) & 1) * 128 + T * 16 + (l & 3) * 4 + e) * 2) = (u16)pkbf(sl * up, 0.f);
    }
    barrier();
    const unsigned ba = L_ACT + (n16 & 1) * 256 + g4 * 16;
    v4u ab[4];
#pragma unroll
    for (int ks = 0; ks < 4; ++ks) ab[ks] = L4(ba + ks * 64);
    v4f d[3];
#pragma unroll
    for (int u = 0; u < 3; ++u) d[u] = v4f{0.f, 0.f, 0.f, 0.f};
#pragma unroll
    for (int ks = 0; ks < 4; ++ks)
#pragma unroll
      for (int u = 0; u < 3; ++u) d[u] = mfma(wf[u][ks], ab[ks], d[u]);
#pragma unroll
    for (int u = 0; u < 3; ++u)
      if (n16 < 2) stc(v4u{tg(d[u][0], tag), tg(d[u][1], tag), tg(d[u][2], tag), tg(d[u][3], tag)}, rs, OFF_P + w2i * 4608 + (3 * w + u) * 128 + n16 * 64 + g4 * 16, 0);
    if (w == 0) STAMP(11);
  }
  asm volatile(".Lkf_last:");
}

extern "C" void kernel_launch(void* const* d_in, const int* in_sizes, int n_in, void* d_out, int out_size, void* d_ws, size_t ws_size, hipStream_t stream) {
  (void)hipFuncSetAttribute((const void*)k_fused, hipFuncAttributeMaxDynamicSharedMemorySize, L_END);
  k_fused<<<256, 768, L_END, stream>>>((const u16*)d_in[0], (const u16*)d_in[1], (const u16*)d_in[3], (unsigned*)d_ws, (const u16*)d_in[2], (const u16*)d_in[4],
                                       (const u16*)d_in[6], (const u16*)d_in[7], (const u16*)d_in[8], (u16*)d_out
  );
}
```

```cpp
#include <hip/hip_runtime.h>
#include <stdint.h>

typedef unsigned short u16;
typedef unsigned long long u64;
typedef unsigned v4u __attribute__((ext_vector_type(4)));
typedef unsigned v2u __attribute__((ext_vector_type(2)));
typedef unsigned v16u __attribute__((ext_vector_type(16)));
typedef float v4f __attribute__((ext_vector_type(4)));
typedef float v2f __attribute__((ext_vector_type(2)));
typedef __bf16 bf8v __attribute__((ext_vector_type(8)));
typedef __bf16 bf2v __attribute__((ext_vector_type(2)));
typedef short s4v __attribute__((ext_vector_type(4)));
typedef __amdgpu_buffer_rsrc_t rsrc_t;
#define GAS __attribute__((address_space(1)))
#define LAS __attribute__((address_space(3)))
#define DI __device__ __forceinline__

#define NFL 4
#define QSTEP 21
#define QCLK 10
#define QCN 4
#define WOSTEP 31
#define GSTEP 31
#define G_T 760
#define WO_FL 8
#define G_FL 8
#define W2_FL 8
#define W2_DL 3
#define YDL 3
#define PDL 10
#define YSENT 4
#define ODIR 256
#define OSLOW 1
#define QP 16, 0, 0, 8, 40
#define OP 64, 0, 0, 3, 12
#define YP 8, 0, 0, 1, 12
#define GP 64, 0, 0, 2, 8
#define PP 16, 0, 0, 0, 10
#define R2N 2
#define R2S 8
#define VIDX 7
#define NPR 4
#define LATE 16
#define VSL 16
#define VSM 8
#define PWS 2
#define SSTEP 18
#define NAE 5
#define XL 0
#define CODE_PIECE 896

constexpr int SK = 32768, WIN = 4096, DM = 576, FF = 4608;
constexpr unsigned TAGM = 0x7ffu;
constexpr int SPIN = 1 << 21;
constexpr int CH = 8192, GSTR = 2176, G_REST = 6656;
constexpr int OFF_Q = 2 * CH, OFF_O = OFF_Q + 16 * CH, OFF_ML = OFF_O + 32 * CH, OFF_Y = OFF_ML + 4 * CH, OFF_G = OFF_Y + 24 * CH, OFF_P = OFF_G + 36 * CH, OFF_CS = OFF_P + 36 * CH,
              OFF_PR = OFF_CS + 14 * CH, OFF_V = 1024, WS_END = OFF_PR + 4 * CH;
__device__ __forceinline__ int cidx(int j) { return (j & ~3) | ((j & 1) << 1) | ((j >> 1) & 1); }
constexpr int L_OB = 98304, L_ML = L_OB + 8192, L_FAC = L_ML + 128, L_QB = L_FAC + 64, L_ACT = L_QB + 512, L_FLG = L_ACT + 2048, L_PF = L_FLG + 64, L_END = L_PF + 1024;
constexpr int L_WQ = 73728, L_X = L_WQ + 18432, L_W2 = 32768;
enum { F_ISS = 0, F_DONE = 1, F_QCNT = 2, F_QRDY = 3, F_WL = 4, F_SM = 5, F_X = 6, F_GO = 7, F_W2 = 8, F_PRG = 9, F_CLK = 10, F_IG = 11, F_POL = 12 };

DI float asf(unsigned v) { return __builtin_bit_cast(float, v); }
DI unsigned asu(float v) { return __builtin_bit_cast(unsigned, v); }
DI unsigned pkbf(float a, float b) { v2f f = {a, b}; return __builtin_bit_cast(unsigned, __builtin_convertvector(f, bf2v)); }
DI float lo16(unsigned v) { return asf(v << 16); }
DI float hi16(unsigned v) { return asf(v & 0xffff0000u); }
DI float rbf(float a) { return lo16(pkbf(a, a)); }
template <int C> DI unsigned dppu(unsigned v) { return (unsigned)__builtin_amdgcn_update_dpp(0, (int)v, C, 0xf, 0xf, true); }
template <int C> DI float dppf(float v) { return asf(dppu<C>(asu(v))); }
constexpr int QX1 = 0xB1, QX2 = 0x4E, HMIR = 0x141, RMIR = 0x140, ROR4 = 0x124, ROR8 = 0x128, NBC = 0x150;
DI float sw32add(float a, float b) { v2u r = __builtin_amdgcn_permlane32_swap(asu(a), asu(b), false, false); return asf(r[0]) + asf(r[1]); }
DI float sw16add(float a, float b) { v2u r = __builtin_amdgcn_permlane16_swap(asu(a), asu(b), false, false); return asf(r[0]) + asf(r[1]); }
DI float sw32max(float a) { v2u r = __builtin_amdgcn_permlane32_swap(asu(a), asu(a), false, false); return fmaxf(asf(r[0]), asf(r[1])); }
DI float sw16max(float a) { v2u r = __builtin_amdgcn_permlane16_swap(asu(a), asu(a), false, false); return fmaxf(asf(r[0]), asf(r[1])); }
DI float rowscatter(float v0, float v1, float v2, float v3) { return sw16add(sw32add(v0, v1), sw32add(v2, v3)); }
DI float rowsum16(float s) { s += dppf<QX1>(s); s += dppf<QX2>(s); s += dppf<HMIR>(s); s += dppf<RMIR>(s); return s; }
DI v4f mfma(v4u a, v4u b, v4f c) { return __builtin_amdgcn_mfma_f32_16x16x32_bf16(__builtin_bit_cast(bf8v, a), __builtin_bit_cast(bf8v, b), c, 0, 0, 0); }
DI unsigned tg(float v, unsigned tag) { return __builtin_amdgcn_bitop3_b32(asu(v), ~TAGM, tag, 0xEA); }
DI v4u tg4(v4f v, unsigned tag) { return v4u{tg(v[0], tag), tg(v[1], tag), tg(v[2], tag), tg(v[3], tag)}; }
DI float untag(unsigned v) { return asf(v & ~TAGM); }
DI u64 bad4(v4u v, unsigned tag) {
  const unsigned a = __builtin_amdgcn_bitop3_b32(v.x, v.y, tag, 0x7E), b = __builtin_amdgcn_bitop3_b32(v.z, v.w, tag, 0x7E);
  return __builtin_amdgcn_ballot_w64(__builtin_amdgcn_bitop3_b32(a, b, TAGM, 0xA8) != 0);
}
DI void stc(v4u v, rsrc_t rs, int voff, int soff) { __builtin_amdgcn_raw_buffer_store_b128(v, rs, voff, soff, 16); }
DI void pload(v4u& g, u64 m, int voff, rsrc_t rs, int soff) {
  asm volatile("s_mov_b64 exec, %2\n\tbuffer_load_dwordx4 %0, %1, %3, %4 offen sc1\n\ts_mov_b64 exec, -1" : "+v"(g) : "v"(voff), "s"(m), "s"(rs), "s"(soff) : "memory");
}
#define PR0 "v[64:67]"
#define PR1 "v[68:71]"
#define PR2 "v[72:75]"
#define PR3 "v[76:79]"
#define PR4 "v[80:83]"
#define PR5 "v[84:87]"
#define PG(j, x) "+{" PR##j "}"(x)
#define PLD(j, v) "buffer_load_dwordx4 " PR##j ", %[" #v "], %[rs], %[s" #j "] offen sc1\n\t"
#define PL(j, v) "s_mov_b64 exec, %[m" #j "]\n\t" PLD(j, v)
#define PCK(j, a, b, c, d) "v_bitop3_b32 %[t0], v" #a ", v" #b ", %[tag] bitop3:0x7e\n\tv_bitop3_b32 %[t1], v" #c ", v" #d ", %[tag] bitop3:0x7e\n\t" \
                           "v_bitop3_b32 %[t0], %[t0], %[t1], %[tm] bitop3:0xa8\n\tv_cmp_ne_u32_e64 %[m" #j "], 0, %[t0]\n\t"
#define PC0 PCK(0, 64, 65, 66, 67)
#define PC1 PCK(1, 68, 69, 70, 71)
#define PC2 PCK(2, 72, 73, 74, 75)
#define PC3 PCK(3, 76, 77, 78, 79)
#define PC4 PCK(4, 80, 81, 82, 83)
#define PC5 PCK(5, 84, 85, 86, 87)
#define POR(a, b) "s_or_b64 %[u], %[" #a "], %[" #b "]\n\t"
#define PBEG "s_branch 2f\n1:\n\t"
template <int TH_, int Q_, int WS_, int P_, int WF_> struct Pace { static constexpr int TH = TH_, Q = Q_, WS = WS_, P = P_, WF = WF_; };
#define PMID "s_mov_b64 exec, -1\n\ts_bcnt1_i32_b64 vcc_lo, %[u]\n\ts_cmp_gt_u32 vcc_lo, %[th]\n\ts_cbranch_scc0 4f\n\ts_waitcnt vmcnt(%[ws])\n\ts_sleep %[q]\n\ts_branch 2f\n" \
             "4:\n\ts_sleep %[p]\n\ts_waitcnt vmcnt(%[wf])\n2:\n\t"
#define PEND "s_cbranch_scc0 3f\n\ts_sub_u32 %[n], %[n], 1\n\ts_cbranch_scc0 1b\n3:"
#define PTMP u64 m0_, m1_, m2_, m3_, m4_, u_; unsigned t0_, t1_, n_ = SPIN
#define POUT [t0] "=&v"(t0_), [t1] "=&v"(t1_), [n] "+s"(n_), [u] "=&s"(u_)
#define PM(j) [m##j] "=&s"(m##j##_)
#define PIN(T) [rs] "s"(rs), [tag] "s"(tag), [tm] "s"(TAGM), [th] "n"(T::TH), [q] "n"(T::Q), [ws] "n"(T::WS), [p] "n"(T::P), [wf] "n"(T::WF)
#define PCLOB "vcc", "scc", "memory"
template <class P> DI void poll2(v4u& g0, v4u& g1, int va, int vb, rsrc_t rs, unsigned tag) {
  PTMP;
  asm volatile(PBEG PL(0, va) PL(1, vb) PMID PC0 PC1 POR(m0, m1) PEND : PG(0, g0), PG(1, g1), PM(0), PM(1), POUT
               : [va] "v"(va), [vb] "v"(vb), [s0] "s"(0), [s1] "s"(0), PIN(P) : PCLOB);
}
template <class P> DI void poll6(v4u* g, int va, rsrc_t rs, unsigned tag) {
  PTMP; u64 m5_;
  asm volatile(PBEG PL(0, va) PL(1, va) PL(2, va) PL(3, va) PL(4, va) PL(5, va) PMID PC0 PC1 PC2 PC3 PC4 PC5 POR(m0, m1) POR(u, m2) POR(u, m3) POR(u, m4) POR(u, m5) PEND
               : PG(0, g[0]), PG(1, g[1]), PG(2, g[2]), PG(3, g[3]), PG(4, g[4]), PG(5, g[5]), PM(0), PM(1), PM(2), PM(3), PM(4), PM(5), POUT
               : [va] "v"(va), [s0] "s"(0), [s1] "s"(1024), [s2] "s"(2048), [s3] "s"(3072), [s4] "s"(4096), [s5] "s"(5120), PIN(P) : PCLOB);
}
template <class P> DI void poll4(v4u* g, int va, int st, rsrc_t rs, unsigned tag) {
  PTMP;
  asm volatile(PBEG PL(0, va) PL(1, va) PL(2, va) PL(3, va) PMID PC0 PC1 PC2 PC3 POR(m0, m1) POR(u, m2) POR(u, m3) PEND
               : PG(0, g[0]), PG(1, g[1]), PG(2, g[2]), PG(3, g[3]), PM(0), PM(1), PM(2), PM(3), POUT
               : [va] "v"(va), [s0] "s"(0), [s1] "s"(st), [s2] "s"(2 * st), [s3] "s"(3 * st), PIN(P) : PCLOB);
}
template <class P> DI void poll5(v4u* g, int va, int st, rsrc_t rs, unsigned tag) {
  PTMP;
  asm volatile(PBEG PL(0, va) PL(1, va) PL(2, va) PL(3, va) PL(4, va) PMID PC0 PC1 PC2 PC3 PC4 POR(m0, m1) POR(u, m2) POR(u, m3) POR(u, m4) PEND
               : PG(0, g[0]), PG(1, g[1]), PG(2, g[2]), PG(3, g[3]), PG(4, g[4]), PM(0), PM(1), PM(2), PM(3), PM(4), POUT
               : [va] "v"(va), [s0] "s"(OFF_P - OFF_G), [s1] "s"(OFF_P - OFF_G + st), [s2] "s"(OFF_P - OFF_G + 2 * st), [s3] "s"(OFF_P - OFF_G + 3 * st), [s4] "s"(G_REST), PIN(P) : PCLOB);
}
#define PLS(j) "s_mov_b64 vcc, %[m" #j "]\n\ts_bitcmp1_b32 vcc_lo, 0\n\ts_cselect_b32 exec_lo, 1, vcc_lo\n\ts_bitcmp1_b32 vcc_hi, 0\n\ts_cselect_b32 exec_hi, 1, vcc_hi\n\t" PLD(j, va)
#define PCNT(j) "s_bcnt1_i32_b64 vcc_lo, %[m" #j "]\n\ts_add_u32 %[c], %[c], vcc_lo\n\t"
template <int D, int Q> DI void pollsent(v4u* g, v4u& k, int va, int st, int vb, rsrc_t rs, unsigned tag) {
  PTMP; unsigned c_;
  asm volatile(PLD(4, vb) PLD(0, va) PLD(1, va) PLD(2, va) PLD(3, va) "s_branch 2f\n1:\n\ts_sleep %[q]\n\t" PL(4, vb) PLS(0) PLS(1) PLS(2) PLS(3) "s_mov_b64 exec, -1\n2:\n\ts_waitcnt vmcnt(0)\n\t"
               PC4 PC0 PC1 PC2 PC3 "s_bcnt1_i32_b64 %[c], %[m4]\n\ts_lshl_b32 %[c], %[c], 8\n\t" PCNT(0) PCNT(1) PCNT(2) PCNT(3) "s_cmp_gt_u32 %[c], %[d]\n\t" PEND
               : PG(0, g[0]), PG(1, g[1]), PG(2, g[2]), PG(3, g[3]), PG(4, k), PM(0), PM(1), PM(2), PM(3), PM(4), POUT, [c] "=&s"(c_)
               : [va] "v"(va), [vb] "v"(vb), [s0] "s"(0), [s1] "s"(st), [s2] "s"(2 * st), [s3] "s"(3 * st), [s4] "s"(0), [rs] "s"(rs), [tag] "s"(tag), [tm] "s"(TAGM), [d] "n"(D), [q] "n"(Q) : PCLOB);
}
#define PDRAIN(...) asm volatile("s_waitcnt vmcnt(0) ; PDRAIN" : __VA_ARGS__::"memory")
#define WAITV(n) asm volatile("s_waitcnt vmcnt(%0)" ::"n"(n) : "memory")
#define WAITL() asm volatile("s_waitcnt lgkmcnt(0)" ::: "memory")
#define SB() __builtin_amdgcn_sched_barrier(0)
#define MEMB() asm volatile("" ::: "memory")
#define FRESH(x) asm volatile("" : "+v"(x))
#define DRAIN(x) asm volatile("s_waitcnt vmcnt(0)" : "+v"(x)::"memory")
#define SLEEP(n) do { if (n) __builtin_amdgcn_s_sleep(n); } while (0)
#define L4(o) (*(LAS v4u*)(lds + (o)))
#define L2_(o) (*(LAS v2u*)(lds + (o)))
template <int NF> DI void stage(u64& src, unsigned& dst, u64 dA, u64 dB, unsigned eA, unsigned eB, int n, unsigned voff) {
  unsigned keep;
  asm volatile("s_mov_b32 %[k], m0\n\ts_mov_b64 s[60:61], %[s]\n"
               "1:\n\ts_mov_b32 m0, %[d]\n\ts_add_i32 %[d], %[d], %[eA]\n\tglobal_load_lds_dwordx4 %[v], s[60:61] nt\n\t"
               "s_add_u32 s60, s60, %[a0]\n\ts_addc_u32 s61, s61, %[a1]\n\ts_waitcnt vmcnt(%[f])\n\t"
               "s_mov_b32 m0, %[d]\n\ts_add_i32 %[d], %[d], %[eB]\n\tglobal_load_lds_dwordx4 %[v], s[60:61] nt\n\t"
               "s_add_u32 s60, s60, %[b0]\n\ts_addc_u32 s61, s61, %[b1]\n\ts_add_i32 %[n], %[n], -1\n\ts_cmp_gt_i32 %[n], 0\n\ts_waitcnt vmcnt(%[f])\n\ts_cbranch_scc1 1b\n\t"
               "s_mov_b64 %[s], s[60:61]\n\ts_mov_b32 m0, %[k]"
               : [k] "=&s"(keep), [s] "+s"(src), [d] "+s"(dst), [n] "+s"(n)
               : [v] "v"(voff), [a0] "s"((unsigned)dA), [a1] "s"((unsigned)(dA >> 32)), [b0] "s"((unsigned)dB), [b1] "s"((unsigned)(dB >> 32)), [eA] "s"(eA), [eB] "s"(eB),
                 [f] "n"(NF - 1)
               : "s60", "s61", "scc", "memory");
}
DI v4u trfrag(LAS char* lds, unsigned a) {
  s4v r0 = __builtin_amdgcn_ds_read_tr16_b64_v4i16((LAS s4v*)(lds + a));
  s4v r1 = __builtin_amdgcn_ds_read_tr16_b64_v4i16((LAS s4v*)(lds + a + 512));
  v2u x = __builtin_bit_cast(v2u, r0), y = __builtin_bit_cast(v2u, r1);
  return v4u{x.x, x.y, y.x, y.y};
}
DI unsigned lflag(LAS char* lds, int f) { return *(volatile LAS unsigned*)(lds + L_FLG + 4 * f); }
DI void lsignal(int f) {
  asm volatile("s_waitcnt lgkmcnt(0)\n\ts_mov_b64 exec, 1\n\tds_add_u32 %0, %1\n\ts_mov_b64 exec, -1" ::"v"(L_FLG + 4 * f), "v"(1) : "memory");
}
DI unsigned larrive(int f) {
  unsigned old;
  asm volatile("s_waitcnt lgkmcnt(0)\n\ts_mov_b64 exec, 1\n\tds_add_rtn_u32 %0, %1, %2\n\ts_mov_b64 exec, -1\n\ts_waitcnt lgkmcnt(0)" : "=&v"(old) : "v"(L_FLG + 4 * f), "v"(1) : "memory");
  return __builtin_amdgcn_readfirstlane(old);
}
template <int SL> DI void lwait(LAS char* lds, int f, unsigned n) { while (lflag(lds, f) < n) { SLEEP(SL); } MEMB(); }
template <int SL> DI int polwait(LAS char* lds) { unsigned v; while (!(v = lflag(lds, F_POL))) { SLEEP(SL); } MEMB(); return __builtin_amdgcn_readfirstlane(v) >> 1; }
DI void barrier() { WAITL(); __builtin_amdgcn_s_barrier(); MEMB(); }
constexpr u64 BCOLS = 0x0003000300030003ull;
#define RD(n, o) "ds_read_b128 %" #n ", %[a] offset:" #o "\n\t"
#define RDON "s_mov_b64 exec, %[m]\n\t"
#define RDOFF "s_mov_b64 exec, -1"
#define LWAITN(n, x) asm volatile("s_waitcnt lgkmcnt(%1)" : "+v"(x) : "n"(n))

#define PTR(T, v, i) ((const GAS T*)(((u64)(v)[(i) + 1] << 32) | (v)[i]))

__global__ void __launch_bounds__(768, 3) k_fused(const u16* x_, const u16* K_, const u16* wq_, unsigned* ws_, const u16* V_, const u16* wo_, const u16* w1_, const u16* w3_,
                                                  const u16* w2_, u16* out_) {
  extern __shared__ __attribute__((aligned(16))) char smem[];
  LAS char* lds = (LAS char*)smem;
  v16u ka; v4u kb;
  asm volatile(".Lkf_first:");
  const int blk = blockIdx.x, t = threadIdx.x, w = __builtin_amdgcn_readfirstlane(t >> 6), l = t & 63;
  const int xcd = blk & 7, idx = blk >> 3;
  const int g4 = l >> 4, n16 = l & 15;
  const int hk = idx & 15, bb = idx >> 4, sl = xcd;
#define ROLES() \
  const int rho = (idx + 4 * xcd) & 31, od = vx & 1, nA = od ? 36 - NAE : NAE; \
  const bool isA = rho < nA, isG = !isA && rho + od < 32; \
  const int ai = (od ? 2 * NAE : 0) + rho * 2 + ((vx >> 1) & 1); \
  const int ks = (xcd >> 2) * 8 + (ai & 7), nt = ai >> 3, half = (ai >> 2) & 1, w2i = nt * 8 + (xcd & 4) + (ai & 3); \
  const int gi = (od ? 4 * (32 - NAE) : 0) + (rho - nA) * 4 + (vx >> 1), cb = gi / 3, kq = gi - 3 * cb
#define MASKS() \
  unsigned mk[4]; \
  _Pragma("unroll") for (int r = 0; r < 4; ++r) mk[r] = ((n16 & 7) >> 1) == r ? ((n16 & 1) ? 0xffff0000u : 0x0000ffffu) : 0u
  constexpr float LOG2E = 1.4426950408889634f;
  unsigned ep;
  int pol, vx = xcd;
  unsigned xr0 = 0, xr1 = 0, xr2 = 0;
#define ARGS_LOAD() asm volatile("s_load_dwordx16 %0, %2, 0x0\n\ts_load_dwordx4 %1, %2, 0x40" : "=&s"(ka), "=&s"(kb) : "s"(__builtin_amdgcn_kernarg_segment_ptr()))
#define ARGS_READY() asm volatile("s_waitcnt lgkmcnt(0)" : "+s"(ka), "+s"(kb)::"memory")
#define EPOCH() asm volatile("s_load_dword %0, %1, 0x0" : "=s"(ep) : "s"(PTR(unsigned, ka, 6)))
#define EPOCH_READY() asm volatile("s_waitcnt lgkmcnt(0)" : "+s"(ep)::"memory")

  if (__builtin_expect(w < 8, 1)) {
    ARGS_LOAD();
    asm volatile("s_mov_b64 exec, %2\n\tds_write_b32 %0, %1\n\ts_mov_b64 exec, -1" ::"v"(L_FLG + 4 * l), "v"(0), "s"(w == 0 ? 0xffffull : 0ull) : "memory");
    const u64 kvb = (((u64)(bb * 16 + hk) * SK + (SK - WIN) + sl * 512) * 128 + w * 512) * 2;
    const unsigned koff = ((l >> 2) & 3) * 256 + (g4 * 4 + (l & 3)) * 16, voff = l * 16;
    unsigned so = ((blk * 5 + idx * 3) & 15) * 8192;
    const int qi = sl + 8 * bb;
    const u64 qb = ((u64)(144 * (qi >> 2) + 8 * w) * 4096 + hk * 256 + (qi & 3) * 64) * 2;
    const unsigned qoff = (l >> 3) * 8192 + (l & 7) * 16;
    const unsigned o3 = w < 2 ? qoff + 128 * 8192 : (w - 2) * 1024 + voff, d3 = w < 2 ? L_WQ + (16 + w) * 1024 : L_X + (w - 2) * 1024;
    const u64 m3 = w < 4 ? ~0ull : w == 4 ? 0xffffull : 0ull;
    ARGS_READY();
    {
      const u64 qp = (u64)PTR(char, ka, 4) + qb, p3 = w < 2 ? qp : (u64)PTR(char, ka, 0);
      asm volatile("s_mov_b32 m0, %[d3]\n\ts_cmp_lt_u32 %[w], 2\n\ts_cbranch_scc1 1f\n\ts_mov_b64 exec, %[m3]\n\tglobal_load_lds_dwordx4 %[o3], %[p3]\n\ts_mov_b64 exec, -1\n\ts_branch 2f\n"
                   "1:\n\tglobal_load_lds_dwordx4 %[o3], %[p3] nt\n2:\n\t"
                   "s_mov_b32 m0, %[d1]\n\ts_nop 0\n\tglobal_load_lds_dwordx4 %[o1], %[qp] nt\n\t"
                   "s_mov_b32 m0, %[d2]\n\ts_nop 0\n\tglobal_load_lds_dwordx4 %[o2], %[qp] nt"
                   ::[w] "s"(w), [d3] "s"(d3), [m3] "s"(m3), [o3] "v"(o3), [p3] "s"(p3), [d1] "s"(L_WQ + w * 1024), [o1] "v"(qoff), [qp] "s"(qp), [d2] "s"(L_WQ + w * 1024 + 8192), [o2] "v"(qoff + 64 * 8192)
                   : "m0", "scc", "memory");
    }
    __builtin_amdgcn_s_barrier();
    const rsrc_t rk = __builtin_amdgcn_make_buffer_rsrc((void*)(PTR(char, ka, 2) + kvb), 0, 0x20000, 0x00020000);
    const rsrc_t rv = __builtin_amdgcn_make_buffer_rsrc((void*)(PTR(char, ka, 8) + kvb), 0, 0x20000, 0x00020000);
    v4u a[32];
#define KVLOAD(i) do { a[i] = __builtin_amdgcn_raw_buffer_load_b128((i) < 16 ? rk : rv, (i) < 16 ? koff : voff, so, 2); \
                       asm volatile("s_addk_i32 %0, 0x2000\n\ts_bitset0_b32 %0, 17" : "+s"(so)::"scc"); } while (0)
#pragma unroll
    for (int i = 0; i < NFL; ++i) { KVLOAD(i); SB(); }
    WAITV(NFL);
    lsignal(F_X);
    EPOCH();
    __builtin_amdgcn_s_setprio(1);
    MASKS();
    v4u q0, q1; float sc[8]; unsigned pp[8]; float mx = 0.f, ls = 0.f; v4f oa = {0.f, 0.f, 0.f, 0.f};
    unsigned tag = 0;
    rsrc_t rs;
    auto qk = [&](int i) {
      v4f d = mfma(a[2 * i], q0, v4f{0.f, 0.f, 0.f, 0.f});
      d = mfma(a[2 * i + 1], q1, d);
      sc[i] = ((dppf<0x00>(d[0]) + dppf<0x55>(d[1])) + dppf<0xAA>(d[2])) + dppf<0xFF>(d[3]);
    };
    auto pv = [&](int j) {
      const unsigned own = pp[j >> 1], oth = dppu<HMIR>(own);
      const unsigned x0 = (j & 1) ? oth : own, x1 = (j & 1) ? own : oth;
      oa = mfma(v4u{x0 & mk[0], x0 & mk[1], x1 & mk[2], x1 & mk[3]}, a[16 + j], oa);
    };
    auto phase = [&](int k) {
      if (k == 0) {
        lwait<1>(lds, F_QRDY, 1);
        EPOCH_READY();
        tag = (ep + 1) & TAGM;
        rs = __builtin_amdgcn_make_buffer_rsrc((void*)PTR(char, ka, 6), 0, WS_END, 0x00020000);
        const v4u qf = L4(L_QB + ((n16 >> 3) * 128 + 32 * g4 + 8 * (n16 & 3)) * 2);
        const bool x1 = (n16 >> 2) & 1;
        const v4u z = {0, 0, 0, 0};
        q0 = x1 ? z : qf; q1 = x1 ? qf : z;
        if (XL) {
          ROLES();
          const int e4 = ((g4 & 1) << 1) | (g4 >> 1);
          const unsigned xa = L_X + (isA ? ((l >> 1) & 1) * DM + w2i * 8 + (l & 1) * 4 + e4 : ((l >> 2) & 1) * DM + (kq * 12 + w * 3) * 16 + (l & 3) * 4 + e4) * 2;
          xr0 = *(LAS u16*)(lds + xa); xr1 = *(LAS u16*)(lds + xa + 32); xr2 = *(LAS u16*)(lds + xa + 64);
        }
#pragma unroll
        for (int p = 0; p < 4; ++p) qk(p);
      } else if (k == 1) {
#pragma unroll
        for (int p = 4; p < 8; ++p) qk(p);
      } else if (k == 2) {
#pragma unroll
        for (int p = 0; p < 8; p += 2) {
          const unsigned u = pkbf(sc[p], sc[p + 1]);
          const unsigned v = pkbf(lo16(u) * 0.08838834764831845f, hi16(u) * 0.08838834764831845f);
          sc[p] = lo16(v); sc[p + 1] = hi16(v);
        }
        mx = fmaxf(fmaxf(fmaxf(sc[0], sc[1]), fmaxf(sc[2], sc[3])), fmaxf(fmaxf(sc[4], sc[5]), fmaxf(sc[6], sc[7])));
        mx = fmaxf(mx, dppf<HMIR>(mx)); mx = sw16max(mx); mx = sw32max(mx);
      } else if (k == 3) {
#pragma unroll
        for (int p = 0; p < 8; ++p) { const float e = __builtin_amdgcn_exp2f((sc[p] - mx) * LOG2E); ls += e; pp[p] = pkbf(e, e); }
        ls += dppf<HMIR>(ls); ls = sw16add(ls, ls); ls = sw32add(ls, ls);
        if ((l & 55) == 0) L2_(L_ML + w * 16 + (l >> 3) * 8) = v2u{asu(mx), asu(ls)};
        lsignal(F_SM);
      }
    };
    constexpr int PV0 = QSTEP + 4 < 32 ? QSTEP + 4 : 32, NPV = 16 - NFL;
#pragma unroll
    for (int i = NFL; i < 32; ++i) {
      WAITV(NFL - 1);
      KVLOAD(i);
      SB();
      if (i == QCLK) lsignal(F_CLK);
      if (i == SSTEP && (idx & 7) == 3) lsignal(F_PRG);
      if (i == WOSTEP) lsignal(F_ISS);
      if (i == GSTEP) lsignal(F_IG);
      if (i >= QSTEP && i <= QSTEP + 3) phase(i - QSTEP);
      if (i >= PV0) {
#pragma unroll
        for (int j = NPV * (i - PV0) / (32 - PV0 + (PV0 == 32)); j < NPV * (i - PV0 + 1) / (32 - PV0 + (PV0 == 32)); ++j) pv(j);
      }
      SB();
    }
    int oo = OFF_O + (bb * 16 + (hk & 7) * 2) * CH + sl * 1024 + l * 16, oa_ = L_OB + w * 1024 + l * 16, or_ = L_OB + l * 16, of_ = L_FAC + (l >> 5) * 32;
    asm volatile("" : "+v"(oo), "+v"(oa_), "+v"(or_), "+v"(of_));
#pragma unroll
    for (int k = 32 - QSTEP; k <= 3; ++k) phase(k);
#pragma unroll
    for (int j = PV0 == 32 ? 0 : NPV; j < 16; ++j) pv(j);
    L4(oa_) = __builtin_bit_cast(v4u, oa);
    unsigned old, pl; v4u o8[8], f0u, f1u;
    asm volatile("s_mov_b64 exec, 1\n\tds_add_rtn_u32 %[old], %[fa], %[one] offset:%[fd]\n\ts_mov_b64 exec, -1\n\tds_read_b32 %[pl], %[fa] offset:%[fp]\n\t"
                 "ds_read_b128 %[f0], %[fc]\n\tds_read_b128 %[f1], %[fc] offset:16\n\t" RD(0, 0) RD(1, 1024) RD(2, 2048) RD(3, 3072) RD(4, 4096) RD(5, 5120) RD(6, 6144) RD(7, 7168) "s_waitcnt lgkmcnt(0)"
                 : "=&v"(o8[0]), "=&v"(o8[1]), "=&v"(o8[2]), "=&v"(o8[3]), "=&v"(o8[4]), "=&v"(o8[5]), "=&v"(o8[6]), "=&v"(o8[7]), [old] "=&v"(old), [pl] "=&v"(pl), [f0] "=&v"(f0u), [f1] "=&v"(f1u)
                 : [fa] "v"(L_FLG), [one] "v"(1), [fc] "v"(of_), [a] "v"(or_), [fd] "n"(4 * F_DONE), [fp] "n"(4 * F_POL) : "memory");
    old = __builtin_amdgcn_readfirstlane(old);
    pl = __builtin_amdgcn_readfirstlane(pl);
    pol = pl >> 1;
    if (__builtin_expect(!pl, 0)) { pol = polwait<PWS>(lds); f0u = L4(L_FAC + (l >> 5) * 32); f1u = L4(L_FAC + (l >> 5) * 32 + 16); }
    vx = pol >> 1; pol &= 1;
    if (old == 7) {
      const v4f f0 = __builtin_bit_cast(v4f, f0u), f1 = __builtin_bit_cast(v4f, f1u);
      v4f o = __builtin_bit_cast(v4f, o8[0]) * f0[0];
#pragma unroll
      for (int i = 1; i < 8; ++i) o = __builtin_elementwise_fma(__builtin_bit_cast(v4f, o8[i]), (v4f)(i < 4 ? f0[i] : f1[i - 4]), o);
      stc(tg4(o, tag), rs, oo, ((hk >> 3) ^ pol) * CH);
    }
    __builtin_amdgcn_s_setprio(0);
  } else {
    ARGS_LOAD();
    __builtin_amdgcn_s_setprio(3);
    __builtin_amdgcn_s_barrier();
    const int aw = w - 8, qi = sl + 8 * bb;
    {
      u64 pc; unsigned back, len;
      asm volatile("s_getpc_b64 %0\n1:\n\ts_mov_b32 %1, 1b-.Lkf_first\n\ts_mov_b32 %2, .Lkf_last-.Lkf_first\n\ts_max_i32 %2, %2, .Lkf_aux-.Lkf_first" : "=s"(pc), "=s"(back), "=s"(len)::"scc");
      pc -= back;
      const unsigned po = idx * CODE_PIECE + l * 16;
      const u64 m = __builtin_amdgcn_ballot_w64(aw == 3 && l < CODE_PIECE / 16 && po + 16 <= len);
      asm volatile("s_mov_b32 m0, %3\n\ts_mov_b64 exec, %2\n\tglobal_load_lds_dwordx4 %0, %1\n\ts_mov_b64 exec, -1" ::"v"(po), "s"(pc), "s"(m), "n"(L_PF) : "m0", "memory");
    }
    const unsigned xo = L_X + (((l >> 3) & 1) * DM + (144 * (qi >> 2) + 8 * aw + 2 * g4)) * 2, xs = L_X + (l >> 5) * (DM * 2) + (l & 31) * 4;
    ARGS_READY();
    EPOCH();
    const int pro = OFF_PR + (aw >> 1) * 2 * CH + (xcd * 2 + (aw & 1)) * 512;
    MASKS();
    lwait<1>(lds, F_X, 8);
    const unsigned tb = (unsigned)__builtin_amdgcn_s_memrealtime();
    v2f xv[9]; v4u a[5]; unsigned xp[5];
#pragma unroll
    for (int i = 0; i < 9; ++i) { const unsigned u = *(LAS unsigned*)(lds + xs + i * 128); xv[i] = v2f{lo16(u), hi16(u)}; }
#pragma unroll
    for (int i = 0; i < 5; ++i) { xp[i] = *(LAS unsigned*)(lds + xo + i * 64); a[i] = L4(L_WQ + aw * 1024 + l * 16 + ((i < 4 || aw < 2) ? i * 4096 : 0)); }
    const v2f s2 = (((xv[0] + xv[1]) + (xv[2] + xv[3])) + ((xv[4] + xv[5]) + (xv[6] + xv[7]))) + xv[8];
    float s = rowsum16(s2[0] + s2[1]); s = sw16add(s, s);
    const float mean = s * (1.f / DM);
    v2f va = {0.f, 0.f}, vb = {0.f, 0.f};
#pragma unroll
    for (int i = 0; i < 9; ++i) { const v2f d = xv[i] - mean; if (i & 1) vb = __builtin_elementwise_fma(d, d, vb); else va = __builtin_elementwise_fma(d, d, va); }
    va += vb;
    float vs = rowsum16(va[0] + va[1]); vs = sw16add(vs, vs);
    const float mr = rbf(mean), dn = rbf(__builtin_amdgcn_sqrtf(rbf(rbf(vs * (1.f / DM)) + 1e-5f)));
    const bool b1 = (l >> 3) & 1;
    const unsigned m0_ = __builtin_amdgcn_readlane(asu(mr), 0), m1_ = __builtin_amdgcn_readlane(asu(mr), 32);
    const unsigned d0_ = __builtin_amdgcn_readlane(asu(dn), 0), d1_ = __builtin_amdgcn_readlane(asu(dn), 32);
    const float mS = asf(b1 ? m1_ : m0_), dS = asf(b1 ? d1_ : d0_);
    const float rS = __builtin_amdgcn_rcpf(dS);
    v4f de = {0.f, 0.f, 0.f, 0.f}, dd = {0.f, 0.f, 0.f, 0.f};
#pragma unroll
    for (int i = 0; i < 5; ++i) {
      const unsigned u = pkbf(lo16(xp[i]) - mS, hi16(xp[i]) - mS);
      const float n0 = lo16(u), n1 = hi16(u);
      float q0 = n0 * rS, q1 = n1 * rS;
      q0 = fmaf(fmaf(-q0, dS, n0), rS, q0); q1 = fmaf(fmaf(-q1, dS, n1), rS, q1);
      unsigned pe = pkbf(q0, q0), po = pkbf(q1, q1);
      if (i == 4 && aw >= 2) { pe = 0; po = 0; }
      de = mfma(v4u{pe & mk[0], pe & mk[1], pe & mk[2], pe & mk[3]}, a[i], de);
      dd = mfma(v4u{po & mk[0], po & mk[1], po & mk[2], po & mk[3]}, a[i], dd);
    }
    v4f r;
#pragma unroll
    for (int cc = 0; cc < 4; ++cc) r[cc] = de[cc] + dppf<ROR8>(dd[cc]);
    if (n16 < 8) L4(L_ACT + ((aw * 2 + (g4 >> 1)) * 64 + 8 * n16 + 4 * (g4 & 1)) * 4) = __builtin_bit_cast(v4u, r);
    const unsigned old = larrive(F_QCNT);
    EPOCH_READY();
    const unsigned tag = (ep + 1) & TAGM;
    const rsrc_t rs = __builtin_amdgcn_make_buffer_rsrc((void*)PTR(char, ka, 6), 0, WS_END, 0x00020000);
    if (idx == VIDX && old == 0) {
      if (l < NPR * 16) stc((v4u)l, rs, pro + ((l >> 3) & 1) * CH + (l >> 4) * 128 + (l & 7) * 16, 0);
      SLEEP(LATE);
      unsigned l0 = ~0u, l1 = ~0u;
      u64 pa = (u64)PTR(char, ka, 6) + pro;
#pragma nounroll
      for (int i = 0; i < NPR; ++i, pa += 128) {
        u64 t0, t1, t2; unsigned v;
        asm volatile("s_memtime %0\n\ts_waitcnt lgkmcnt(0)\n\ts_load_dword %3, %4, 0 glc\n\ts_waitcnt lgkmcnt(0)\n\ts_memtime %1\n\ts_waitcnt lgkmcnt(0)\n\t"
                     "s_load_dword %3, %4, %5 glc\n\ts_waitcnt lgkmcnt(0)\n\ts_memtime %2\n\ts_waitcnt lgkmcnt(0)"
                     : "=&s"(t0), "=&s"(t1), "=&s"(t2), "=&s"(v) : "s"(pa), "n"(CH) : "memory");
        l0 = min(l0, (unsigned)t1 - (unsigned)t0); l1 = min(l1, (unsigned)t2 - (unsigned)t1);
      }
      const unsigned vote = tag | (((l0 > l1) ^ (xcd >> 2)) << 31);
      asm volatile("s_mov_b64 exec, 1\n\tbuffer_store_dwordx4 %0, %1, %2, 0 offen sc1\n\ts_mov_b64 exec, -1" ::"v"((v4u)vote), "v"(OFF_V + xcd * 16), "s"(rs) : "memory");
    }
    if ((idx & 7) == 3 && old == 1) {
      lwait<2>(lds, F_PRG, 8);
      const unsigned pr = tag | ((unsigned)__builtin_amdgcn_s_memrealtime() << 11);
      asm volatile("s_mov_b64 exec, 1\n\tbuffer_store_dwordx4 %0, %1, %2, 0 offen sc1\n\ts_mov_b64 exec, -1" ::"v"((v4u)pr), "v"(OFF_V + 128 + (xcd * 4 + (idx >> 3)) * 16), "s"(rs) : "memory");
    }
    if (old == 3) {
      v4f p[4];
#pragma unroll
      for (int i = 0; i < 4; ++i) p[i] = __builtin_bit_cast(v4f, L4(L_ACT + i * 512 + (l & 31) * 16));
      if (l < 32) stc(tg4((p[0] + p[1]) + (p[2] + p[3]), tag), rs, OFF_Q + hk * CH + qi * 512 + l * 16, 0);
      lwait<2>(lds, F_CLK, QCN);
      const int off = OFF_Q + hk * CH + g4 * 512 + bb * 256 + n16 * 16;
      v4u g[4];
#pragma unroll
      for (int j = 0; j < 4; ++j) g[j] = (v4u)(~tag);
      poll4<Pace<QP>>(g, off, 2048, rs, tag);
      float qv[4];
#pragma unroll
      for (int cc = 0; cc < 4; ++cc) qv[cc] = (untag(g[0][cc]) + untag(g[1][cc])) + (untag(g[2][cc]) + untag(g[3][cc]));
      L2_(L_QB + l * 8) = v2u{pkbf(qv[0], qv[1]), pkbf(qv[2], qv[3])};
      WAITL();
      if (l == 0) *(volatile LAS unsigned*)(lds + L_FLG + 4 * F_QRDY) = 1;
      PDRAIN(PG(0, g[0]), PG(1, g[1]), PG(2, g[2]), PG(3, g[3]));
    }
    auto mlpub = [&]() {
      lwait<2>(lds, F_SM, VSM);
      v4u vt = l < 40 ? (v4u)(~tag) : (v4u)tag;
      u64 mv = 0xff;
      asm volatile("s_mov_b64 exec, %3\n\tbuffer_load_dwordx4 v[64:67], %1, %2, 0 offen sc1\n\ts_mov_b64 exec, -1" : PG(0, vt) : "v"(OFF_V + l * 16), "s"(rs), "s"(0xffffffffffull) : "memory");
      lwait<2>(lds, F_SM, 8);
      const v2u ml = L2_(L_ML + (l & 7) * 16 + ((l >> 3) & 1) * 8);
      const float m = asf(ml.x);
      float M = fmaxf(m, dppf<QX1>(m)); M = fmaxf(M, dppf<QX2>(M)); M = fmaxf(M, dppf<HMIR>(M));
      const float e = __builtin_amdgcn_exp2f((m - M) * LOG2E);
      float L = e * asf(ml.y); L += dppf<QX1>(L); L += dppf<QX2>(L); L += dppf<HMIR>(L);
      if (l < 16) *(LAS float*)(lds + L_FAC + l * 4) = e;
      if (l < 16 && (l & 7) < 4) stc(v4u{tg(M, tag), tg(L, tag), tag, tag}, rs, OFF_ML + (bb * 16 + hk) * 1024 + sl * 128 + (l >> 3) * 16 + (l & 7) * 32, 0);
      asm volatile("s_waitcnt vmcnt(1) ; PDRAIN" : PG(0, vt)::"memory");
#pragma nounroll
      for (int s = 0; s < SPIN; ++s) {
        mv = bad4(vt, tag);
        if (!mv) break;
        SLEEP(VSL);
        pload(vt, mv, OFF_V + l * 16, rs, 0);
        DRAIN(vt);
      }
      const int pol = __builtin_popcountll(__builtin_amdgcn_ballot_w64((int)vt.x < 0) & 0xff) > 4;
      int d = (int)(vt.x - __builtin_amdgcn_readlane(vt.x, 8)) >> 11;
      d += (int)dppu<QX1>(d); d += (int)dppu<QX2>(d);
      const int me = __builtin_amdgcn_readlane(d, 8 + 4 * xcd), o = (l - 8) >> 2;
      const int rk = __builtin_popcountll(__builtin_amdgcn_ballot_w64(l >= 8 && l < 40 && !(l & 3) && !((o ^ xcd) & 4) && (d < me || (d == me && o < xcd))));
      const int v = (xcd & 4) | ((rk & 1) << 1) | (rk >> 1);
      asm volatile("ds_write_b32 %0, %1" ::"v"(L_FLG + 4 * F_POL), "v"(1 + 2 * pol + 4 * v) : "memory");
    };
    if (aw == 1) mlpub();
    pol = polwait<1>(lds); vx = pol >> 1; pol &= 1;
    ROLES();
    __builtin_amdgcn_s_setprio(0);
    const unsigned sw = ((l & 7) ^ (((l >> 3) & 2) | ((aw & 1) << 2))) * 16;
    if (isA) {
      u64 src = (u64)PTR(char, ka, 10) + ((u64)(ks * 256 + aw * 8) * DM + nt * 64) * 2;
      unsigned dst = aw * 1024;
      lwait<2>(lds, F_ISS, 8);
      stage<WO_FL>(src, dst, 32 * DM * 2, 32 * DM * 2, 4096, 4096, 4, (l >> 3) * (DM * 2) + sw);
      WAITV(0);
      lsignal(F_WL);
    } else if (isG) {
      const u64 pw1 = (u64)PTR(char, ka, 12), dA = (u64)PTR(char, ka, 14) - pw1;
      u64 src = pw1 + ((u64)(kq * 192 + (aw & 1) * 8) * FF + cb * 128 + (aw >> 1) * 64) * 2;
      unsigned dst = (aw >> 1) * 24576 + (aw & 1) * 1024;
      const unsigned vo = (l >> 3) * (FF * 2) + sw;
      lwait<2>(lds, F_IG, 8);
      while ((unsigned)__builtin_amdgcn_s_memrealtime() - tb < G_T) __builtin_amdgcn_s_sleep(2);
      stage<G_FL>(src, dst, dA, 16 * FF * 2 - dA, 49152, 2048 - 49152, 12, vo);
      WAITV(0);
      lsignal(F_WL);
    }
    asm volatile(".Lkf_aux:");
  }

  ROLES();
  const unsigned tag = (ep + 1) & TAGM;
  const rsrc_t rs = __builtin_amdgcn_make_buffer_rsrc((void*)PTR(char, ka, 6), 0, WS_END, 0x00020000);
  const GAS u16* x = PTR(u16, ka, 0);
  const int aw = w - 8;
  const int tq = (l >> 2) & 3, tp = l & 3;
  const unsigned trf = ((tq >> 1) | ((g4 & 1) << 1)) << 1;
  const unsigned trow = (8 * g4 + tq) * 128 + (tp & 1) * 8;
  auto traddr = [&](int tt) { return trow + (((2 * tt + (tp >> 1)) ^ trf) * 16); };
  const int e4 = ((g4 & 1) << 1) | (g4 >> 1);
  const int near = (xcd >> 2) ^ pol;
  if (isA) {
    if (w >= 8) {
      {
        v4u wf[8], bf[8];
        lwait<1>(lds, F_WL, 4);
        const unsigned ad = traddr(aw);
#pragma unroll
        for (int k = 0; k < 8; ++k) wf[k] = trfrag(lds, ad + k * 4096);
        const unsigned ba = L_ACT + (n16 & 1) * 512 + g4 * 16;
        const int yt = nt * 4 + aw, y3 = yt / 3;
        int yo = OFF_Y + y3 * 2 * CH + (yt - 3 * y3) * 2048 + ks * 128 + (n16 >> 1) * CH + (n16 & 1) * 64 + g4 * 16;
        asm volatile("" : "+v"(wf[0]), "+v"(wf[1]), "+v"(wf[2]), "+v"(wf[3]), "+v"(wf[4]), "+v"(wf[5]), "+v"(wf[6]), "+v"(wf[7]), "+v"(yo));
        barrier();
        asm volatile(RDON RD(0, 0) RD(1, 64) RD(2, 128) RD(3, 192) RD(4, 256) RD(5, 320) RD(6, 384) RD(7, 448) RDOFF
                     : "=&v"(bf[0]), "=&v"(bf[1]), "=&v"(bf[2]), "=&v"(bf[3]), "=&v"(bf[4]), "=&v"(bf[5]), "=&v"(bf[6]), "=&v"(bf[7])
                     : [a] "v"(ba), [m] "s"(0x000f000f000f000full) : "memory");
        v4f d0 = {0.f, 0.f, 0.f, 0.f}, d1 = {0.f, 0.f, 0.f, 0.f};
#define MM(k) LWAITN(6 - (k), bf[k]); LWAITN(6 - (k), bf[k + 1]); d0 = mfma(wf[k], bf[k], d0); d1 = mfma(wf[k + 1], bf[k + 1], d1); SB();
        MM(0) MM(2) MM(4) MM(6)
#undef MM
        if (n16 < 4) stc(tg4(d0 + d1, tag), rs, yo, 0);
      }
      v4u g = l < 48 ? (v4u)(~tag) : (v4u)tag, st = g, cv = g;
      const int T = (w2i & 1) * 4 + aw, pbase = OFF_G + (cidx(w2i >> 1) ^ pol) * CH + g4 * GSTR;
      const int co = OFF_CS + ((w2i >> 1) * 3 + g4) * 1024 + ((T * 2 + ((l >> 3) & 1)) * 4 + (l & 3)) * 16;
      const int go = pbase + T * 256 + n16 * 16, so = pbase + 2048 + ((l >> 2) & 1) * 16;
      {
        u64 src = (u64)PTR(char, kb, 0) + ((u64)(w2i * 64 + aw * 8) * DM + half * 320) * 2;
        unsigned dst = L_W2 + aw * 1024;
        for (int i = 0; i < W2_DL; ++i) __builtin_amdgcn_s_sleep(4);
        stage<W2_FL>(src, dst, 32 * DM * 2, 128 - 32 * DM * 2, 4096, 4096, 5 - half, (l >> 3) * (DM * 2) + ((l & 7) ^ (((l >> 3) & 2) | ((aw & 1) << 2))) * 16);
        asm volatile("s_mov_b64 exec, %7\n\tbuffer_load_dwordx4 v[72:75], %3, %6, 0 offen sc1\n\tbuffer_load_dwordx4 v[64:67], %4, %6, 0 offen sc1\n\t"
                       "buffer_load_dwordx4 v[68:71], %5, %6, 0 offen sc1\n\ts_mov_b64 exec, -1"
                       : PG(2, cv), PG(0, g), PG(1, st) : "v"(co), "v"(go), "v"(so), "s"(rs), "s"(0xffffffffffffull) : "memory");
#pragma unroll
          for (int i = 0; i < R2N; ++i)
            asm volatile("s_sleep %6\n\ts_mov_b64 exec, %5\n\tbuffer_load_dwordx4 v[64:67], %2, %4, 0 offen sc1\n\tbuffer_load_dwordx4 v[68:71], %3, %4, 0 offen sc1\n\ts_mov_b64 exec, -1"
                         : PG(0, g), PG(1, st) : "v"(go), "v"(so), "s"(rs), "s"(0xffffffffffffull), "n"(R2S) : "memory");
          asm volatile("s_waitcnt vmcnt(%3)" : PG(2, cv), PG(0, g), PG(1, st) : "n"(2 * R2N + 2) : "memory");
        lsignal(F_W2);
      }
#pragma nounroll
      for (int s = 0; s < SPIN; ++s) {
        const u64 mc = bad4(cv, tag);
        if (!mc) break;
        SLEEP(4);
        pload(cv, mc, co, rs, 0);
        DRAIN(cv);
      }
      const v4f cf = __builtin_bit_cast(v4f, cv & ~TAGM);
      float CS = rowscatter(cf[0], cf[1], cf[2], cf[3]);
      unsigned aa = L_ACT + 1024 + (((l >> 2) & 1) * 64 + aw * 16 + (l & 3) * 4 + e4) * 2;
      asm volatile("" : "+v"(CS), "+v"(aa));
      poll2<Pace<GP>>(g, st, go, so, rs, tag);
      const float c0 = untag(st.x), mq = c0 + untag(st.y);
      float m = sw16add(mq, mq); m = sw32add(m, m); m *= (1.f / 3.f);
      const float dm = g4 < 3 ? mq - m : 0.f;
      float m2 = fmaf(192.f * dm, dm, untag(st.z)); m2 = sw16add(m2, m2); m2 = sw32add(m2, m2);
      const float mr = rbf(m), dn = rbf(__builtin_amdgcn_sqrtf(rbf(rbf(m2 * (1.f / DM)) + 1e-5f)));
      const v4f v = __builtin_elementwise_fma((v4f)c0, cf, __builtin_bit_cast(v4f, g & ~TAGM));
      float val = rowscatter(v[0], v[1], v[2], v[3]);
      val = rbf(fmaf(-mr, CS, val) * __builtin_amdgcn_rcpf(dn));
      const float up = dppf<ROR8>(val);
      const float sl = rbf(val * __builtin_amdgcn_rcpf(1.f + __builtin_amdgcn_exp2f(-val * LOG2E)));
      if (!(l & 8)) *(LAS u16*)(lds + aa) = (u16)pkbf(sl * up, 0.f);
      barrier();
      PDRAIN(PG(0, g), PG(1, st));
    } else {
      const bool red = half && w == 7;
      const int col = ((l >> 1) & 1) * DM + w2i * 8 + (l & 1) * 4 + e4;
      float h = 0.f;
      if (w < 4) {
        constexpr int OSTR = 1024;
        const int so = OFF_O + ((w >> 1) * 16 + (ks & 7) * 2 + near) * CH;
        const int mo = OFF_ML + ((w >> 1) * 16 + ks) * 1024 + ((l & 7) ^ (l >> 5)) * 128 + (w & 1) * 16, po = so + (w & 1) * 512 + (l >> 5) * OSTR + (l & 31) * 16;
        unsigned la = L_ACT + (w * 128 + 8 * n16 + 4 * (g4 & 1) + 2 * (l >> 5)) * 2;
        v4u ml = (v4u)(~tag), g[4];
#pragma unroll
        for (int j = 0; j < 4; ++j) g[j] = (v4u)(~tag);
        pollsent<ODIR, OSLOW>(g, ml, po, 2 * OSTR, mo, rs, tag);
        const float Ms = untag(ml.x);
        float M = fmaxf(Ms, dppf<QX1>(Ms)); M = fmaxf(M, dppf<QX2>(M)); M = fmaxf(M, dppf<HMIR>(M));
        const float e = __builtin_amdgcn_exp2f((Ms - M) * LOG2E);
        float L = e * untag(ml.y); L += dppf<QX1>(L); L += dppf<QX2>(L); L += dppf<HMIR>(L);
        const float wt = e * __builtin_amdgcn_rcpf(L);
        float ww[4] = {dppf<NBC>(wt), dppf<NBC + 2>(wt), dppf<NBC + 4>(wt), dppf<NBC + 6>(wt)};
        asm volatile("" : "+v"(ww[0]), "+v"(ww[1]), "+v"(ww[2]), "+v"(ww[3]), "+v"(la));
        poll4<Pace<OP>>(g, po, 2 * OSTR, rs, tag);
        v4f o = __builtin_bit_cast(v4f, g[0] & ~TAGM) * ww[0];
#pragma unroll
        for (int j = 1; j < 4; ++j) o = __builtin_elementwise_fma(__builtin_bit_cast(v4f, g[j] & ~TAGM), (v4f)ww[j], o);
        *(LAS unsigned*)(lds + la) = pkbf(sw32add(o[0], o[2]), sw32add(o[1], o[3]));
        barrier();
        PDRAIN(PG(0, g[0]), PG(1, g[1]), PG(2, g[2]), PG(3, g[3]), PG(4, ml));
      } else {
        barrier();
        if (red) {
          const unsigned xr = XL ? xr0 : x[col];
          v4u y = (v4u)(~tag);
          const int yt = w2i >> 1, y3 = yt / 3;
          const int yo = OFF_Y + (y3 * 2 + near) * CH + (yt - 3 * y3) * 2048 + (l >> 2) * 128 + ((l >> 1) & 1) * 64 + (w2i & 1) * 32 + (l & 1) * 16;
          u64 my = ~0ull;
#pragma nounroll
          for (int s = 0; s < SPIN; ++s) {
            SLEEP(8);
            pload(y, my, yo, rs, 0);
            DRAIN(y);
            my = bad4(y, tag);
            if (!my) break;
          }
          float hs = rowscatter(untag(y.x), untag(y.y), untag(y.z), untag(y.w));
          hs += dppf<ROR4>(hs); hs += dppf<ROR8>(hs);
          h = rbf(lo16(xr) + rbf(hs));
        }
      }
      const bool t3 = w < 4 && !half;
      v4u wf[3][2], bf[2];
      lwait<2>(lds, F_W2, 4);
      const unsigned ad = L_W2 + (w >> 2) * 8192 + traddr(w & 3);
#pragma unroll
      for (int u = 0; u < 3; ++u) { const unsigned a = ad + (u < 2 || t3 ? u * 16384 : 0); wf[u][0] = trfrag(lds, a); wf[u][1] = trfrag(lds, a + 4096); }
      const unsigned ba = L_ACT + 1024 + (n16 & 1) * 128 + g4 * 16;
      int po = (w2i < 64 ? OFF_P + w2i * 128 : OFF_G + G_REST + (w2i - 64) * 128) + (cidx(half * 20 + w) ^ pol) * CH + (g4 >> 1) * 64 + n16 * 32 + (g4 & 1) * 16;
      asm volatile("" : "+v"(wf[0][0]), "+v"(wf[0][1]), "+v"(wf[1][0]), "+v"(wf[1][1]), "+v"(wf[2][0]), "+v"(wf[2][1]), "+v"(po), "+v"(h));
      barrier();
      asm volatile(RDON RD(0, 0) RD(1, 64) RDOFF : "=&v"(bf[0]), "=&v"(bf[1]) : [a] "v"(ba), [m] "s"(BCOLS) : "memory");
      LWAITN(1, bf[0]);
      v4f d[3];
#pragma unroll
      for (int u = 0; u < 3; ++u) d[u] = mfma(wf[u][0], bf[0], v4f{0.f, 0.f, 0.f, 0.f});
      SB();
      LWAITN(0, bf[1]);
#pragma unroll
      for (int u = 0; u < 3; ++u) d[u] = mfma(wf[u][1], bf[1], d[u]);
      if (n16 < 2) {
        stc(tg4(d[0], tag), rs, po, 0); stc(tg4(d[1], tag), rs, po, 8 * CH);
        if (t3) stc(tg4(d[2], tag), rs, po, 16 * CH);
      }
      if (red) {
        const int off = OFF_G + (cidx(w2i >> 1) ^ pol) * CH + (l >> 2) * 128 + (w2i & 1) * 64 + (l & 3) * 16;
        v4u p[5];
#pragma unroll
        for (int j = 0; j < 5; ++j) p[j] = (j < 4 || l < 32) ? (v4u)(~tag) : (v4u)tag;
        SLEEP(PDL);
        poll5<Pace<PP>>(p, off, 2048, rs, tag);
        v4f v[5];
#pragma unroll
        for (int j = 0; j < 5; ++j) v[j] = __builtin_bit_cast(v4f, p[j] & ~TAGM);
        const v4f vs = ((v[0] + v[1]) + (v[2] + v[3])) + v[4];
        float r = rowscatter(vs[0], vs[1], vs[2], vs[3]);
        r += dppf<ROR4>(r); r += dppf<ROR8>(r);
        const unsigned res = pkbf(h + rbf(r), 0.f);
        if (!(l & 12)) ((GAS u16*)PTR(u16, kb, 2))[col] = (u16)res;
        if (w2i == 0 && l == 0) __builtin_amdgcn_raw_buffer_store_b32(ep + 1, rs, 0, 0, 16);
        PDRAIN(PG(0, p[0]), PG(1, p[1]), PG(2, p[2]), PG(3, p[3]), PG(4, p[4]));
      }
    }
  } else if (isG) {
    const int gb = OFF_G + (cidx(cb) ^ pol) * CH + kq * GSTR;
    if (w >= 4) {
      const int wv = w - 4;
      v4u wf[2][6], bf[6]; v4f cs[2];
      lwait<1>(lds, F_WL, 4);
      const v4u ones = {0x3f803f80u, 0x3f803f80u, 0x3f803f80u, 0x3f803f80u};
      const int go = gb + wv * 256;
#pragma unroll
      for (int u = 0; u < 2; ++u) {
        const unsigned ad = (u * 2 + (wv >> 2)) * 24576 + traddr(wv & 3);
#pragma unroll
        for (int ks = 0; ks < 6; ++ks) wf[u][ks] = trfrag(lds, ad + ks * 4096);
        v4f cc = {0.f, 0.f, 0.f, 0.f};
#pragma unroll
        for (int ks = 0; ks < 6; ++ks) cc = mfma(wf[u][ks], ones, cc);
        cs[u] = cc;
      }
      if (n16 < 2) stc(tg4(n16 ? cs[1] : cs[0], tag), rs, OFF_CS + gi * 1024 + ((wv * 2 + n16) * 4 + g4) * 16, 0);
      const unsigned bc = L_ACT + (n16 & 1) * 384, ba = bc + g4 * 16;
      unsigned c0u;
      barrier();
      asm volatile(RDON RD(0, 0) RD(1, 64) RD(2, 128) RD(3, 192) RD(4, 256) RD(5, 320) "ds_read_u16 %6, %[c]\n\t" RDOFF
                   : "=&v"(bf[0]), "=&v"(bf[1]), "=&v"(bf[2]), "=&v"(bf[3]), "=&v"(bf[4]), "=&v"(bf[5]), "=&v"(c0u)
                   : [a] "v"(ba), [c] "v"(bc), [m] "s"(BCOLS) : "memory");
      v4f d0 = {0.f, 0.f, 0.f, 0.f}, d1 = {0.f, 0.f, 0.f, 0.f};
#define MM(k) LWAITN(6 - (k), bf[k]); d0 = mfma(wf[0][k], bf[k], d0); d1 = mfma(wf[1][k], bf[k], d1); SB();
      MM(0) MM(1) MM(2) MM(3) MM(4) MM(5)
#undef MM
      LWAITN(0, c0u);
      const v4f nc = (v4f)(-lo16(c0u));
      if (n16 < 2) {
        stc(tg4(__builtin_elementwise_fma(nc, cs[0], d0), tag), rs, go + (n16 * 4 + g4) * 16, 0);
        stc(tg4(__builtin_elementwise_fma(nc, cs[1], d1), tag), rs, go + 128 + (n16 * 4 + g4) * 16, 0);
      }
    } else {
      const int cg = kq * 12 + w * 3;
      float xv[3];
#pragma unroll
      for (int u = 0; u < 3; ++u) xv[u] = lo16(XL ? (u == 0 ? xr0 : u == 1 ? xr1 : xr2) : x[((l >> 2) & 1) * DM + (cg + u) * 16 + (l & 3) * 4 + e4]);
      asm volatile("" : "+v"(xv[0]), "+v"(xv[1]), "+v"(xv[2]));
      const int off = OFF_Y + ((kq * 4 + w) * 2 + near) * CH + l * 16;
      v4u g[6];
#pragma unroll
      for (int u = 0; u < 6; ++u) g[u] = (v4u)(~tag);
      for (int i = 0; i < YDL; ++i) __builtin_amdgcn_s_sleep(16);
#pragma nounroll
      for (int s = 0; s < SPIN; ++s) {
        pload(g[0], ~0ull, off, rs, 0);
        DRAIN(g[0]);
        if (~bad4(g[0], tag)) break;
        SLEEP(YSENT);
      }
      poll6<Pace<YP>>(g, off, rs, tag);
#pragma unroll
      for (int u = 0; u < 3; ++u) {
        const v4f y = __builtin_bit_cast(v4f, g[2 * u] & ~TAGM) + __builtin_bit_cast(v4f, g[2 * u + 1] & ~TAGM);
        float hs = rowscatter(y[0], y[1], y[2], y[3]);
        hs += dppf<ROR8>(hs);
        const unsigned h = pkbf(xv[u] + rbf(hs), 0.f);
        if (!(l & 8)) *(LAS u16*)(lds + L_ACT + (((l >> 2) & 1) * 192 + (w * 3 + u) * 16 + (l & 3) * 4 + e4) * 2) = (u16)h;
      }
      barrier();
      if (w == 0) {
        const unsigned ba = L_ACT + (l >> 5) * 384;
        const bool on = (l & 31) < 24;
        const v4u hv = L4(ba + (on ? (l & 31) : 0) * 16);
        const float c0 = lo16(*(LAS u16*)(lds + ba));
        v2f s2 = {0.f, 0.f}, q2 = {0.f, 0.f};
#pragma unroll
        for (int i = 0; i < 4; ++i) { const v2f d = v2f{lo16(hv[i]), hi16(hv[i])} - c0; s2 += d; q2 = __builtin_elementwise_fma(d, d, q2); }
        float s = on ? s2[0] + s2[1] : 0.f, q = on ? q2[0] + q2[1] : 0.f;
        s = rowsum16(s); q = rowsum16(q); s = sw16add(s, s); q = sw16add(q, q);
        const float dl = s * (1.f / 192.f), m2 = fmaxf(fmaf(-s, dl, q), 0.f);
        if ((l & 31) < 4) stc(v4u{tg(c0, tag), tg(dl, tag), tg(m2, tag), tag}, rs, gb + 2048 + (l >> 5) * 16 + (l & 3) * 32, 0);
      }
      PDRAIN(PG(0, g[0]), PG(1, g[1]), PG(2, g[2]), PG(3, g[3]), PG(4, g[4]), PG(5, g[5]));
    }
  }
  asm volatile(".Lkf_last:");
}

extern "C" void kernel_launch(void* const* d_in, const int* in_sizes, int n_in, void* d_out, int out_size, void* d_ws, size_t ws_size, hipStream_t stream) {
  char* wsb = (char*)(((uintptr_t)d_ws + 0x1fffff) & ~(uintptr_t)0x1fffff);
  (void)hipFuncSetAttribute((const void*)k_fused, hipFuncAttributeMaxDynamicSharedMemorySize, L_END);
  k_fused<<<256, 768, L_END, stream>>>((const u16*)d_in[0], (const u16*)d_in[1], (const u16*)d_in[3], (unsigned*)wsb, (const u16*)d_in[2], (const u16*)d_in[4],
                                       (const u16*)d_in[6], (const u16*)d_in[7], (const u16*)d_in[8], (u16*)d_out);
}
```
